```python
import torch

def _softmax(x, axis=-1):
    return torch.softmax(x, dim=axis)
def _gelu(x):
    return torch.nn.functional.gelu(x, approximate="tanh")
def _silu(x):
    return torch.nn.functional.silu(x)
def _sigmoid(x):
    return torch.sigmoid(x)
def _layernorm(x, axis=-1, eps=1e-5):
    m = x.mean(dim=axis, keepdim=True)
    v = x.var(dim=axis, keepdim=True, unbiased=False)
    return (x - m) / torch.sqrt(v + eps)
def _scatter_add(tgt, idx, src, axis=0):
    return tgt.clone().index_add_(axis, idx, src)
def _segment_sum(src, seg, n):
    out = torch.zeros((n,) + src.shape[1:], dtype=src.dtype)
    return out.index_add_(0, seg, src)
def _pad(x, pw):
    flat = tuple(v for pair in reversed(pw) for v in pair)
    return torch.nn.functional.pad(x, flat)

kernel_name = 'recurrent_scan__sliding_block_cached__geglu_ffn__decode'

def setup_inputs():

    _w = (lambda a: (lambda t: t.to(torch.bfloat16) if t.is_floating_point() else t)(torch.as_tensor(a)))
    def _A_setup():
        B, Sq, D = 4, 1, 576
        Hq, Hkv, Sk, Dh = 32, 16, 8192, 128
        assert Hq % Hkv == 0, (Hq, Hkv)
        return {
            "x": _w((lambda *s: torch.randn(*s, dtype=torch.bfloat16) * 0.05)(B, Sq, D)),
            "k_cache": _w((lambda *s: torch.randn(*s, dtype=torch.bfloat16) * 0.8)(B, Hkv, Sk, Dh)),
            "v_cache": _w((lambda *s: torch.randn(*s, dtype=torch.bfloat16) * 0.2)(B, Hkv, Sk, Dh)),
            "wq": _w((lambda *s: torch.randn(*s, dtype=torch.bfloat16) * 0.8)(D, Hq * Dh) * (D ** -0.5)),
            "wo": _w((lambda *s: torch.randn(*s, dtype=torch.bfloat16) * 0.2)(Hq * Dh, D) * ((Hq * Dh) ** -0.5)),
        }
    _A_in = _A_setup()

    def _B_setup():
        B, S, D, F = 4, 1, 576, 4608
        return {
            "x": _w((lambda *s: torch.randn(*s, dtype=torch.bfloat16) * 0.05)(B, S, D)),
            "w_gate": _w((lambda *s: torch.randn(*s, dtype=torch.bfloat16) * 0.1)(D, F)),
            "w_up": _w((lambda *s: torch.randn(*s, dtype=torch.bfloat16) * 0.1)(D, F)),
            "w_down": _w((lambda *s: torch.randn(*s, dtype=torch.bfloat16) * 0.1)(F, D)),
        }
    _B_in = _B_setup()

    return {
        "A_x": _A_in["x"],
        "A_k_cache": _A_in["k_cache"],
        "A_v_cache": _A_in["v_cache"],
        "A_wq": _A_in["wq"],
        "A_wo": _A_in["wo"],
        "B_x": _B_in["x"],
        "B_w_gate": _B_in["w_gate"],
        "B_w_up": _B_in["w_up"],
        "B_w_down": _B_in["w_down"],
    }

def reference(A_x, A_k_cache, A_v_cache, A_wq, A_wo, B_x, B_w_gate, B_w_up, B_w_down):
    def _A_ref(x, k_cache, v_cache, wq, wo):
        B, Sq, D = x.shape
        Hq, Dh = 32, 128
        Hkv, Sk = k_cache.shape[1], k_cache.shape[2]
        W = min(4096, max(256, Sk // 4))
        rep = Hq // Hkv
        q = (lambda x, *d: x.permute(*d))((x @ wq).reshape(B, Sq, Hq, Dh), 0, 2, 1, 3)
        k = (lambda x, n, axis: torch.repeat_interleave(x, n, dim=axis))(k_cache[:, :, Sk - W:, :], rep, axis=1)
        v = (lambda x, n, axis: torch.repeat_interleave(x, n, dim=axis))(v_cache[:, :, Sk - W:, :], rep, axis=1)
        scores = torch.einsum("bhqd,bhkd->bhqk", q, k) * (Dh ** -0.5)
        p = _softmax(scores, axis=-1)
        o = torch.einsum("bhqk,bhkd->bhqd", p, v)
        return (lambda x, *d: x.permute(*d))(o, 0, 2, 1, 3).reshape(B, Sq, Hq * Dh) @ wo

    def _B_ref(x, w_gate, w_up, w_down):
        return (_gelu(x @ w_gate) * (x @ w_up)) @ w_down

    x = A_x
    xn = _layernorm(x, axis=-1)
    s = _A_ref(xn, A_k_cache, A_v_cache, A_wq, A_wo)
    g = _silu(_B_ref(xn, B_w_gate, B_w_up, B_w_down))
    return x + s * g
```

```cpp
#define PAD 4000
#include <hip/hip_runtime.h>
#include <stdint.h>

typedef unsigned short u16;
typedef unsigned v4u __attribute__((ext_vector_type(4)));
typedef unsigned v2u __attribute__((ext_vector_type(2)));
typedef float f4 __attribute__((ext_vector_type(4)));
typedef short s8v __attribute__((ext_vector_type(8)));
typedef __bf16 bf2v __attribute__((ext_vector_type(2)));
#define LAS __attribute__((address_space(3)))
#define GAS __attribute__((address_space(1)))

constexpr int DM = 576, SK = 8192, WIN = 2048, FF = 4608, NQ = 4096;
constexpr int NCU = 256, NTHR = 768;
constexpr int N_S1 = 208, N_WO = 192;
constexpr int NT = 2, SC1 = 16;
constexpr float QK_C = 0.08838834764831845f * 1.4426950408889634f;

#define WS_BASE 0
constexpr int S1_ROW = (NQ + 2 * FF) * 4;
constexpr int WS_EPOCH = 0, WS_S1 = 4096, WS_H1 = WS_S1 + 4 * S1_ROW, WS_ML = WS_H1 + NCU * 1024,
              WS_H2 = WS_ML + NCU * 128, WS_F2 = WS_H2 + 3 * 64 * 6144, WS_END = WS_F2 + 3 * 16 * 6144;
constexpr int WS_STAMP = 4 << 20;

constexpr int XN_STR = 1168, H_STR = 656;
constexpr int L_XN = 0, L_H = L_XN + 4 * XN_STR, L_Q = L_H + 4 * H_STR, L_S1R = L_Q + 512, L_AM = L_S1R + 3 * 1024,
              L_AL = L_AM + 64, L_AD = L_AL + 64, L_AF = L_AD + 8 * 1024, L_FLAG = L_AF + 1024, L_END = L_FLAG + 64;
enum { F_XN = 0, F_TAG, F_Q, F_H, F_S1, F_ISS, F_M, F_D, F_PUB, F_L, F_ML, F_A, F_H2 };
static_assert(L_H % 16 == 0 && L_Q % 16 == 0 && L_S1R % 16 == 0 && L_AM % 16 == 0 && L_AD % 16 == 0 && L_FLAG % 16 == 0, "align");

#define GU_EARLY 64
#define POLL_SLEEP 1
#define KPAT 0
constexpr unsigned LDS_SPIN = 1u << 22, GLB_SPIN = 1u << 16;

#define STAMP(i) do {} while (0)

__device__ __forceinline__ __amdgpu_buffer_rsrc_t mkrsrc(const void* p, unsigned bytes) {
  return __builtin_amdgcn_make_buffer_rsrc((void*)p, 0, bytes, 0x00020000);
}
template <int AUX> __device__ __forceinline__ v4u ld16(__amdgpu_buffer_rsrc_t r, int voff, int soff = 0) {
  return __builtin_amdgcn_raw_buffer_load_b128(r, voff, soff, AUX);
}
__device__ __forceinline__ void st16_sc1(v4u v, __amdgpu_buffer_rsrc_t r, int voff) {
  __builtin_amdgcn_raw_buffer_store_b128(v, r, voff, 0, SC1);
}
__device__ __forceinline__ f4 mfma(v4u a, v4u b, f4 c) {
  return __builtin_amdgcn_mfma_f32_16x16x32_bf16(__builtin_bit_cast(s8v, a), __builtin_bit_cast(s8v, b), c, 0, 0, 0);
}
__device__ __forceinline__ unsigned cvtpk(float lo, float hi) {
  bf2v r = {(__bf16)lo, (__bf16)hi};
  return __builtin_bit_cast(unsigned, r);
}
__device__ __forceinline__ float rbf(float f) { return (float)(__bf16)f; }
__device__ __forceinline__ float rcp(float f) { return __builtin_amdgcn_rcpf(f); }
__device__ __forceinline__ float blo(unsigned u) { return __uint_as_float(u << 16); }
__device__ __forceinline__ float bhi(unsigned u) { return __uint_as_float(u & 0xffff0000u); }
template <int CTRL> __device__ __forceinline__ float dpp(float x) {
  return __builtin_bit_cast(float, __builtin_amdgcn_mov_dpp(__builtin_bit_cast(int, x), CTRL, 0xf, 0xf, true));
}
constexpr int ROR1 = 0x121, ROR2 = 0x122, ROR4 = 0x124, ROR8 = 0x128;
__device__ __forceinline__ float row_sum(float x) { x += dpp<ROR8>(x); x += dpp<ROR4>(x); x += dpp<ROR2>(x); x += dpp<ROR1>(x); return x; }
__device__ __forceinline__ float row_max(float x) {
  x = fmaxf(x, dpp<ROR8>(x)); x = fmaxf(x, dpp<ROR4>(x)); x = fmaxf(x, dpp<ROR2>(x)); x = fmaxf(x, dpp<ROR1>(x)); return x; }
__device__ __forceinline__ void swap16(float& a, float& b) {
  auto s = __builtin_amdgcn_permlane16_swap(__float_as_uint(a), __float_as_uint(b), false, false);
  a = __uint_as_float(s[0]); b = __uint_as_float(s[1]);
}
__device__ __forceinline__ void swap32(float& a, float& b) {
  auto s = __builtin_amdgcn_permlane32_swap(__float_as_uint(a), __float_as_uint(b), false, false);
  a = __uint_as_float(s[0]); b = __uint_as_float(s[1]);
}
__device__ __forceinline__ float xrow_sum(float x) { float a = x, b = x; swap16(a, b); x = a + b; a = x; b = x; swap32(a, b); return a + b; }
__device__ __forceinline__ float xrow_max(float x) { float a = x, b = x; swap16(a, b); x = fmaxf(a, b); a = x; b = x; swap32(a, b); return fmaxf(a, b); }

__device__ __forceinline__ void lds_wait_ge(volatile LAS unsigned* f, unsigned want) {
#pragma nounroll
  for (unsigned s = 0; s < LDS_SPIN; ++s) {
    if ((unsigned)__builtin_amdgcn_readfirstlane(*f) >= want) break;
    __builtin_amdgcn_s_sleep(1);
  }
  asm volatile("" ::: "memory");
}
__device__ __forceinline__ void lds_wait_fast(volatile LAS unsigned* f, unsigned want) {
#pragma nounroll
  for (unsigned s = 0; s < LDS_SPIN; ++s)
    if ((unsigned)__builtin_amdgcn_readfirstlane(*f) >= want) break;
  asm volatile("" ::: "memory");
}
__device__ __forceinline__ void lgkm0() { asm volatile("s_waitcnt lgkmcnt(0)" ::: "memory"); }
__device__ __forceinline__ unsigned lds_arrive(LAS unsigned* f, int lane) {
  lgkm0();
  unsigned o = 0;
  if (lane == 0) o = __hip_atomic_fetch_add(f, 1u, __ATOMIC_RELAXED, __HIP_MEMORY_SCOPE_WORKGROUP);
  o = __builtin_amdgcn_readfirstlane(o);
  asm volatile("" ::: "memory");
  return o;
}

__device__ __forceinline__ void gemv_group(const v4u* r, v4u a, f4* acc) {
#pragma unroll
  for (int w = 0; w < 4; ++w) {
    v4u lo, hi;
#pragma unroll
    for (int p = 0; p < 4; ++p) {
      lo[p] = __builtin_amdgcn_perm(r[2 * p + 1][w], r[2 * p][w], 0x05040100u);
      hi[p] = __builtin_amdgcn_perm(r[2 * p + 1][w], r[2 * p][w], 0x07060302u);
    }
    acc[2 * w] = mfma(a, lo, acc[2 * w]);
    acc[2 * w + 1] = mfma(a, hi, acc[2 * w + 1]);
  }
}
__device__ __forceinline__ f4 gemv_finish(const f4* acc, int lane) {
  f4 G;
#pragma unroll
  for (int j = 0; j < 4; ++j) {
    float e0 = lane < 32 ? acc[j][0] : acc[j + 4][0], e1 = lane < 32 ? acc[j][1] : acc[j + 4][1];
    float e2 = lane < 32 ? acc[j][2] : acc[j + 4][2], e3 = lane < 32 ? acc[j][3] : acc[j + 4][3];
    swap16(e0, e1); swap16(e2, e3);
    float t0 = e0 + dpp<ROR8>(e1), t1 = dpp<ROR8>(e2) + e3;
    G[j] = (lane & 8) ? t1 : t0;
  }
  return G;
}

__global__ void __launch_bounds__(NTHR, 3)
fused_decode(const u16* __restrict__ x, const u16* __restrict__ kc, const u16* __restrict__ vc, const u16* __restrict__ wq,
             const u16* __restrict__ wo, const u16* __restrict__ wg, const u16* __restrict__ wu, const u16* __restrict__ wd,
             u16* __restrict__ out, unsigned char* __restrict__ ws) {
  __shared__ __attribute__((aligned(16))) unsigned char lds_raw[L_END];
  LAS unsigned char* L = (LAS unsigned char*)lds_raw;
  volatile LAS unsigned* FL = (volatile LAS unsigned*)(L + L_FLAG);
  const int c = blockIdx.x, lane = threadIdx.x & 63, wave = __builtin_amdgcn_readfirstlane(threadIdx.x >> 6);
  const int g = lane >> 4, n = lane & 15;
  const int pair = c >> 2, sp = c & 3;
  const __amdgpu_buffer_rsrc_t rws = mkrsrc(ws, WS_END);
  STAMP(0);
  const unsigned long long tpad0 = __builtin_amdgcn_s_memrealtime();

  if (wave >= 4) {
    const int w = wave - 4, wb = (w + pair) & 7;
    const size_t kvo = ((size_t)pair * SK + (SK - WIN) + sp * 512 + wb * 64) * 128;
    const __amdgpu_buffer_rsrc_t rk = mkrsrc(kc + kvo, 16384), rv = mkrsrc(vc + kvo, 16384);
    const int kvo0 = (n + ((n >> 3) & 1) * 16 + ((n >> 2) & 1) * 32) * 256 + g * 16;
    const int vvo0 = g * 4096 + n * 16;
    v4u K[16], V[16];
    __builtin_amdgcn_sched_barrier(0);
    __builtin_amdgcn_s_barrier();
    __builtin_amdgcn_sched_barrier(0);
#pragma unroll
    for (int i = 0; i < 16; ++i) K[i] = ld16<NT>(rk, (kvo0 ^ ((i >> 2) * 4096)) + (i & 3) * 64);
#pragma unroll
    for (int i = 0; i < 16; ++i) V[i] = ld16<NT>(rv, (vvo0 ^ ((((i >> 3) & 1) | (((i >> 2) & 1) << 1)) * 4096)) + i * 256);
    __builtin_amdgcn_sched_barrier(0);
    if (lane == 0) __hip_atomic_fetch_add((LAS unsigned*)(L + L_FLAG) + F_ISS, 1u, __ATOMIC_RELAXED, __HIP_MEMORY_SCOPE_WORKGROUP);
    STAMP(1);
    lds_wait_ge(FL + F_Q, 1);
    STAMP(2);
    f4 S[4];
#pragma unroll
    for (int t = 0; t < 4; ++t) S[t] = f4{0.f, 0.f, 0.f, 0.f};
#pragma unroll
    for (int ks = 0; ks < 4; ++ks) {
      const v4u qf = *(const LAS v4u*)(L + L_Q + (n & 1) * 256 + ks * 64 + g * 16);
#pragma unroll
      for (int t = 0; t < 4; ++t) S[t] = mfma(qf, K[t * 4 + ks], S[t]);
    }
    const float s0 = g == 0 ? S[0][0] : g == 1 ? S[1][0] : g == 2 ? S[2][0] : S[3][0];
    const float s1 = g == 0 ? S[0][1] : g == 1 ? S[1][1] : g == 2 ? S[2][1] : S[3][1];
    {
      const float m0 = xrow_max(row_max(s0)), m1 = xrow_max(row_max(s1));
      if (lane == 0) { *(LAS float*)(L + L_AM + w * 4) = m0; *(LAS float*)(L + L_AM + 32 + w * 4) = m1; }
    }
    lds_arrive((LAS unsigned*)(L + L_FLAG) + F_M, lane);
    STAMP(3);
    lds_wait_ge(FL + F_M, 8);
    float M0, M1;
    {
      const f4 a = *(const LAS f4*)(L + L_AM), b = *(const LAS f4*)(L + L_AM + 16), d = *(const LAS f4*)(L + L_AM + 32), e = *(const LAS f4*)(L + L_AM + 48);
      M0 = fmaxf(fmaxf(fmaxf(a[0], a[1]), fmaxf(a[2], a[3])), fmaxf(fmaxf(b[0], b[1]), fmaxf(b[2], b[3])));
      M1 = fmaxf(fmaxf(fmaxf(d[0], d[1]), fmaxf(d[2], d[3])), fmaxf(fmaxf(e[0], e[1]), fmaxf(e[2], e[3])));
    }
    const float p0 = __builtin_amdgcn_exp2f((s0 - M0) * QK_C), p1 = __builtin_amdgcn_exp2f((s1 - M1) * QK_C);
    {
      const float l0 = xrow_sum(row_sum(p0)), l1 = xrow_sum(row_sum(p1));
      if (lane == 0) { *(LAS float*)(L + L_AL + w * 4) = l0; *(LAS float*)(L + L_AL + 32 + w * 4) = l1; }
    }
    if (lds_arrive((LAS unsigned*)(L + L_FLAG) + F_L, lane) == 7) {
      const unsigned tag = FL[F_TAG];
      const int h = lane & 1;
      const f4 la = *(const LAS f4*)(L + L_AL + h * 32), lb = *(const LAS f4*)(L + L_AL + h * 32 + 16);
      const float Ls = ((la[0] + la[1]) + (la[2] + la[3])) + ((lb[0] + lb[1]) + (lb[2] + lb[3]));
      if (lane < 2) st16_sc1(v4u{__float_as_uint(h ? M1 : M0), tag, __float_as_uint(Ls), tag}, rws, WS_ML + c * 128 + h * 16);
      FL[F_ML] = 1;
    }
    const unsigned pk = cvtpk(p0, p1);
    const unsigned hs = (n & 8) ? 0x0302u : 0x0100u, one = (n & 1) ? ((hs << 16) | 0x0c0cu) : (0x0c0c0000u | hs);
    unsigned sel[4];
#pragma unroll
    for (int r = 0; r < 4; ++r) sel[r] = ((n & 7) >> 1) == r ? one : 0x0c0c0c0cu;
    f4 O = f4{0.f, 0.f, 0.f, 0.f};
#define PV_STEP(i) { const unsigned pb = (unsigned)__builtin_amdgcn_mov_dpp((int)pk, 0x150 + (i), 0xf, 0xf, true); \
      v4u a; a[0] = __builtin_amdgcn_perm(pb, pb, sel[0]); a[1] = __builtin_amdgcn_perm(pb, pb, sel[1]); \
      a[2] = __builtin_amdgcn_perm(pb, pb, sel[2]); a[3] = __builtin_amdgcn_perm(pb, pb, sel[3]); O = mfma(a, V[i], O); }
    PV_STEP(0) PV_STEP(1) PV_STEP(2) PV_STEP(3) PV_STEP(4) PV_STEP(5) PV_STEP(6) PV_STEP(7)
    PV_STEP(8) PV_STEP(9) PV_STEP(10) PV_STEP(11) PV_STEP(12) PV_STEP(13) PV_STEP(14) PV_STEP(15)
#undef PV_STEP
    *(LAS f4*)(L + L_AD + w * 1024 + lane * 16) = O;
    STAMP(4);
    if (lds_arrive((LAS unsigned*)(L + L_FLAG) + F_D, lane) == 7) {
      const unsigned tag = FL[F_TAG];
      f4 o = *(const LAS f4*)(L + L_AD + lane * 16);
#pragma unroll
      for (int k = 1; k < 8; ++k) o += *(const LAS f4*)(L + L_AD + k * 1024 + lane * 16);
      const int h = g >> 1;
      const f4 la = *(const LAS f4*)(L + L_AL + h * 32), lb = *(const LAS f4*)(L + L_AL + h * 32 + 16);
      const float Ls = ((la[0] + la[1]) + (la[2] + la[3])) + ((lb[0] + lb[1]) + (lb[2] + lb[3]));
      const float inv = rcp(Ls);
      st16_sc1(v4u{cvtpk(o[0] * inv, o[1] * inv), tag, cvtpk(o[2] * inv, o[3] * inv), tag}, rws,
               WS_H1 + c * 1024 + h * 512 + n * 32 + (g & 1) * 16);
      FL[F_PUB] = 1;
      STAMP(5);
    }
    return;
  }

  if (wave == 3) {
    if (lane < 16) FL[lane] = 0;
    const unsigned tag = __hip_atomic_load((GAS unsigned*)(ws + WS_EPOCH), __ATOMIC_RELAXED, __HIP_MEMORY_SCOPE_AGENT) + 1u;
    const __amdgpu_buffer_rsrc_t rx = mkrsrc(x, 4 * DM * 2);
    v4u X[5];
#pragma unroll
    for (int i = 0; i < 5; ++i) X[i] = ld16<0>(rx, (i == 4 && n >= 8) ? 0x40000000 : g * (DM * 2) + (n + 16 * i) * 16);
    lgkm0();
    __builtin_amdgcn_sched_barrier(0);
    __builtin_amdgcn_s_barrier();
    __builtin_amdgcn_sched_barrier(0);
    {
      float xf[40], s = 0.f;
#pragma unroll
      for (int i = 0; i < 5; ++i)
#pragma unroll
        for (int k = 0; k < 4; ++k) { xf[i * 8 + 2 * k] = blo(X[i][k]); xf[i * 8 + 2 * k + 1] = bhi(X[i][k]); }
#pragma unroll
      for (int i = 0; i < 40; ++i) s += xf[i];
      const float mean = row_sum(s) * (1.f / DM);
      float v = 0.f;
#pragma unroll
      for (int i = 0; i < 40; ++i) { const float d = (i >= 32 && n >= 8) ? 0.f : xf[i] - mean; v = fmaf(d, d, v); }
      const float var = row_sum(v) * (1.f / DM);
      const float mb = rbf(mean), rden = rcp(rbf(__builtin_amdgcn_sqrtf(rbf(rbf(var) + 1e-5f))));
#pragma unroll
      for (int i = 0; i < 5; ++i) {
        v4u o;
#pragma unroll
        for (int k = 0; k < 4; ++k) o[k] = cvtpk(rbf(xf[i * 8 + 2 * k] - mb) * rden, rbf(xf[i * 8 + 2 * k + 1] - mb) * rden);
        if (i < 4 || n < 8) *(LAS v4u*)(L + L_XN + g * XN_STR + (n + 16 * i) * 16) = o;
      }
    }
    FL[F_TAG] = tag;
    lgkm0();
    FL[F_XN] = 1;
    STAMP(1);
    {
      const int off = WS_S1 + (pair >> 4) * S1_ROW + (pair & 15) * 1024 + lane * 16;
      v4u d;
      _Pragma("nounroll") for (unsigned s = 0; s < GLB_SPIN; ++s) {
        asm volatile("" ::: "memory");
        d = ld16<SC1>(rws, off);
        if (__all(d[1] == tag && d[3] == tag)) break;
        __builtin_amdgcn_s_sleep(4);
      }
      *(LAS v2u*)(L + L_Q + lane * 8) = v2u{d[0], d[2]};
      lgkm0();
      FL[F_Q] = 1;
    }
    STAMP(2);
    if (c >= N_S1) {
      const int rs = (c - N_S1) / 3;
      v4u G[5], U[5];
      int loff[5];
      _Pragma("nounroll") for (unsigned s = 0; s < GLB_SPIN; ++s) {
        asm volatile("" ::: "memory");
        bool ok = true;
#pragma unroll
        for (int i = 0; i < 5; ++i) {
          const int t = min(lane + 64 * i, 287), b = t / 72, qd = t - b * 72;
          const int off = WS_S1 + b * S1_ROW + (NQ + 288 * rs + 4 * qd) * 4;
          G[i] = ld16<SC1>(rws, off); U[i] = ld16<SC1>(rws, off + FF * 4);
          loff[i] = b * H_STR + qd * 8;
        }
#pragma unroll
        for (int i = 0; i < 5; ++i) ok &= (G[i][1] == tag) & (G[i][3] == tag) & (U[i][1] == tag) & (U[i][3] == tag);
        if (__all(ok)) break;
        __builtin_amdgcn_s_sleep(4);
      }
      if (lane < 32) *(LAS v2u*)(L + L_H + (lane >> 3) * H_STR + 576 + (lane & 7) * 8) = v2u{0u, 0u};
#pragma unroll
      for (int i = 0; i < 5; ++i) {
        float hv[4];
#pragma unroll
        for (int k = 0; k < 4; ++k) {
          const unsigned gw = G[i][(k >> 1) * 2], uw = U[i][(k >> 1) * 2];
          const float a = (k & 1) ? bhi(gw) : blo(gw), u = (k & 1) ? bhi(uw) : blo(uw);
          const float z2 = (-2.f * 0.7978845608028654f * 1.4426950408889634f) * (a + 0.044715f * a * a * a);
          const float ge = a * rcp(1.f + __builtin_amdgcn_exp2f(z2));
          hv[k] = rbf(ge) * u;
        }
        *(LAS v2u*)(L + L_H + loff[i]) = v2u{cvtpk(hv[0], hv[1]), cvtpk(hv[2], hv[3])};
      }
      lgkm0();
      FL[F_H] = 1;
      STAMP(3);
      return;
    }
    if (c >= N_WO) return;
    const int fcs = c / 48, fr = c - fcs * 48, fw = fr >> 4, fpc = (fr >> 3) & 1, fgr = fr & 7;
    const int oidx = (2 * (fgr & 1) + ((fgr >> 1) & 1)) * DM + 64 * (3 * fcs + fw) + 8 * (lane & 7) + 4 * (fgr >> 2) + 2 * fpc;
    const unsigned xres = c < 144 ? *(const unsigned*)(x + oidx) : 0u;
    {
      const int ksl = c / 3, hh = ksl >> 1, pc = (((n & 3) * 16 + (hh >> 1)) * 4 + 2 * (n >> 3));
      const int o1 = WS_H1 + pc * 1024 + (hh & 1) * 512 + (8 * (ksl & 1) + 4 * ((n >> 2) & 1) + g) * 32, o2 = WS_ML + pc * 128 + (hh & 1) * 16;
      v4u A0, A1, B0, B1, Ma, Mb; bool ok = false;
      lds_wait_ge(FL + F_ML, 1);
      _Pragma("nounroll") for (unsigned s = 0; s < GLB_SPIN; ++s) {
        asm volatile("" ::: "memory");
        if (!ok) {
          Ma = ld16<SC1>(rws, o2); Mb = ld16<SC1>(rws, o2 + 128);
          ok = (Ma[1] == tag) & (Ma[3] == tag) & (Mb[1] == tag) & (Mb[3] == tag);
        }
        if (__all(ok)) break;
        __builtin_amdgcn_s_sleep(2);
      }
      const float ma = __uint_as_float(Ma[0]), mb = __uint_as_float(Mb[0]);
      float M = fmaxf(ma, mb); M = fmaxf(M, dpp<ROR8>(M));
      const float ea = __uint_as_float(Ma[2]) * __builtin_amdgcn_exp2f((ma - M) * QK_C), eb = __uint_as_float(Mb[2]) * __builtin_amdgcn_exp2f((mb - M) * QK_C);
      float Ls = ea + eb; Ls += dpp<ROR8>(Ls);
      const float inv = rcp(Ls), wa = ea * inv, wb = eb * inv;
      STAMP(3);
      lds_wait_ge(FL + F_PUB, 1);
      ok = false;
      _Pragma("nounroll") for (unsigned s = 0; s < GLB_SPIN; ++s) {
        asm volatile("" ::: "memory");
        if (!ok) {
          A0 = ld16<SC1>(rws, o1); A1 = ld16<SC1>(rws, o1 + 16); B0 = ld16<SC1>(rws, o1 + 1024); B1 = ld16<SC1>(rws, o1 + 1040);
          ok = (A0[1] == tag) & (A0[3] == tag) & (A1[1] == tag) & (A1[3] == tag) & (B0[1] == tag) & (B0[3] == tag) & (B1[1] == tag) & (B1[3] == tag);
        }
        if (__all(ok)) break;
        __builtin_amdgcn_s_sleep(POLL_SLEEP);
      }
      const unsigned pa[4] = {A0[0], A0[2], A1[0], A1[2]}, pb[4] = {B0[0], B0[2], B1[0], B1[2]};
      v4u a;
#pragma unroll
      for (int k = 0; k < 4; ++k) {
        float lo = fmaf(wa, blo(pa[k]), wb * blo(pb[k])), hi = fmaf(wa, bhi(pa[k]), wb * bhi(pb[k]));
        lo += dpp<ROR8>(lo); hi += dpp<ROR8>(hi);
        a[k] = cvtpk(lo, hi);
      }
      *(LAS v4u*)(L + L_AF + lane * 16) = a;
      lgkm0();
      FL[F_A] = 1;
      STAMP(4);
    }
    if (c >= 144) return;
    {
      const int lo_ = fw * 2048 + fpc * 1024 + fgr * 128 + (lane & 7) * 16;
      const int o1 = WS_H2 + (fcs * 64 + (lane >> 3)) * 6144 + lo_, o2 = WS_F2 + (fcs * 16 + (lane >> 3)) * 6144 + lo_;
      v4u P[10]; bool ok = false;
      lds_wait_fast(FL + F_H2, 3);
      _Pragma("nounroll") for (unsigned s = 0; s < GLB_SPIN; ++s) {
        asm volatile("" ::: "memory");
        if (!ok) {
#pragma unroll
          for (int i = 0; i < 8; ++i) P[i] = ld16<SC1>(rws, o1, i * 8 * 6144);
          P[8] = ld16<SC1>(rws, o2); P[9] = ld16<SC1>(rws, o2, 8 * 6144);
          ok = true;
#pragma unroll
          for (int i = 0; i < 10; ++i) ok &= (P[i][1] == tag) & (P[i][3] == tag);
        }
        if (__all(ok)) break;
        __builtin_amdgcn_s_sleep(POLL_SLEEP);
      }
      STAMP(6);
      float s0 = 0.f, s1 = 0.f;
#pragma unroll
      for (int i = 0; i < 8; ++i) { s0 += __uint_as_float(P[i][0]); s1 += __uint_as_float(P[i][2]); }
      float f0 = __uint_as_float(P[8][0]) + __uint_as_float(P[9][0]), f1 = __uint_as_float(P[8][2]) + __uint_as_float(P[9][2]);
      s0 += dpp<ROR8>(s0); s1 += dpp<ROR8>(s1); f0 += dpp<ROR8>(f0); f1 += dpp<ROR8>(f1);
      s0 = rbf(xrow_sum(s0)); s1 = rbf(xrow_sum(s1)); f0 = rbf(xrow_sum(f0)); f1 = rbf(xrow_sum(f1));
      const float g0 = rbf(f0 * rcp(1.f + __builtin_amdgcn_exp2f(-1.4426950408889634f * f0)));
      const float g1 = rbf(f1 * rcp(1.f + __builtin_amdgcn_exp2f(-1.4426950408889634f * f1)));
      if (lane < 8) *(unsigned*)(out + oidx) = cvtpk(blo(xres) + rbf(s0 * g0), bhi(xres) + rbf(s1 * g1));
      if (c == 0 && lane == 0) __hip_atomic_store((GAS unsigned*)(ws + WS_EPOCH), tag, __ATOMIC_RELAXED, __HIP_MEMORY_SCOPE_AGENT);
      STAMP(7);
      while (__builtin_amdgcn_s_memrealtime() - tpad0 < PAD) __builtin_amdgcn_s_sleep(1);
    }
    return;
  }

  const int w = wave;
  const int rowl = 32 * (n >> 3) + 8 * g, chk = (n & 7) * 16;
  const int afo = ((n >> 2) & 1) * 64 + g * 16;
  f4 acc[8];
#pragma unroll
  for (int j = 0; j < 8; ++j) acc[j] = f4{0.f, 0.f, 0.f, 0.f};
  if (c < N_S1) {
    const u16* base; int stride, ug;
    if (c < 64) { const int u = (c & 7) * 8 + (c >> 3); ug = u; base = wq + u * 64; stride = NQ * 2; }
    else { const int c2 = c - 64, u = (c2 & 7) * 18 + (c2 >> 3); ug = 64 + u; base = (u < 72 ? wg + u * 64 : wu + (u - 72) * 64); stride = FF * 2; }
    const __amdgpu_buffer_rsrc_t rw = mkrsrc(base, (DM - 1) * stride + 128);
    const int voff = rowl * stride + chk;
    v4u R[24];
    if (c >= GU_EARLY) __builtin_amdgcn_s_barrier();
    __builtin_amdgcn_sched_barrier(0);
#pragma unroll
    for (int i = 0; i < 24; ++i) R[i] = ld16<NT>(rw, voff, (192 * w + 64 * (i >> 3) + (i & 7)) * stride);
    __builtin_amdgcn_sched_barrier(0);
    if (c < GU_EARLY) __builtin_amdgcn_s_barrier();
    __builtin_amdgcn_sched_barrier(0);
    STAMP(1);
    lds_wait_ge(FL + F_XN, 1);
#pragma unroll
    for (int gi = 0; gi < 3; ++gi) {
      const v4u a = *(const LAS v4u*)(L + L_XN + (n & 3) * XN_STR + (3 * w + gi) * 128 + afo);
      gemv_group(R + 8 * gi, a, acc);
    }
    *(LAS f4*)(L + L_S1R + w * 1024 + lane * 16) = gemv_finish(acc, lane);
    if (lds_arrive((LAS unsigned*)(L + L_FLAG) + F_S1, lane) == 2) {
      const unsigned tag = FL[F_TAG];
      const f4 o = (*(const LAS f4*)(L + L_S1R + lane * 16) + *(const LAS f4*)(L + L_S1R + 1024 + lane * 16)) + *(const LAS f4*)(L + L_S1R + 2048 + lane * 16);
      st16_sc1(v4u{cvtpk(o[0], o[1]), tag, cvtpk(o[2], o[3]), tag}, rws,
               WS_S1 + (2 * (n >> 3) + (g & 1)) * S1_ROW + (64 * ug + 8 * (n & 7) + 4 * (g >> 1)) * 4);
    }
    STAMP(2);
    if (c >= N_WO) return;
    const int cs = c % 3, ksl = c / 3;
    const unsigned tag = FL[F_TAG];
    lds_wait_ge(FL + F_ISS, 8);
    const __amdgpu_buffer_rsrc_t ro = mkrsrc(wo + (size_t)ksl * 64 * DM + (3 * cs + w) * 64, 63 * DM * 2 + 128);
    v4u W[8];
#pragma unroll
    for (int i = 0; i < 8; ++i) W[i] = ld16<NT>(ro, rowl * (DM * 2) + chk + i * (DM * 2));
    v4u T[8];
#pragma unroll
    for (int q = 0; q < 4; ++q)
#pragma unroll
      for (int p = 0; p < 4; ++p) {
        T[2 * q][p] = __builtin_amdgcn_perm(W[2 * p + 1][q], W[2 * p][q], 0x05040100u);
        T[2 * q + 1][p] = __builtin_amdgcn_perm(W[2 * p + 1][q], W[2 * p][q], 0x07060302u);
      }
    asm volatile("" : "+v"(T[0]), "+v"(T[1]), "+v"(T[2]), "+v"(T[3]), "+v"(T[4]), "+v"(T[5]), "+v"(T[6]), "+v"(T[7]));
    STAMP(3);
    lds_wait_fast(FL + F_A, 1);
    const v4u a = *(const LAS v4u*)(L + L_AF + lane * 16);
#pragma unroll
    for (int j = 0; j < 8; ++j) acc[j] = mfma(a, T[j], f4{0.f, 0.f, 0.f, 0.f});
    const f4 G = gemv_finish(acc, lane);
    const int rec = WS_H2 + (cs * 64 + ksl) * 6144 + w * 2048 + lane * 16;
    st16_sc1(v4u{__float_as_uint(G[0]), tag, __float_as_uint(G[1]), tag}, rws, rec);
    st16_sc1(v4u{__float_as_uint(G[2]), tag, __float_as_uint(G[3]), tag}, rws, rec + 1024);
    if (lane == 0) __hip_atomic_fetch_add((LAS unsigned*)(L + L_FLAG) + F_H2, 1u, __ATOMIC_RELAXED, __HIP_MEMORY_SCOPE_WORKGROUP);
    STAMP(5);
  } else {
    __builtin_amdgcn_s_barrier();
    const int cs = (c - N_S1) % 3, rs = (c - N_S1) / 3;
    const __amdgpu_buffer_rsrc_t rd = mkrsrc(wd + (size_t)rs * 288 * DM + (3 * cs + w) * 64, 287 * DM * 2 + 128);
    const int voff = rowl * (DM * 2) + chk, voff4 = n >= 8 ? 0x40000000 : voff;
    lds_wait_ge(FL + F_ISS, 8);
    v4u R[24];
#pragma unroll
    for (int i = 0; i < 24; ++i) R[i] = ld16<NT>(rd, voff, (64 * (i >> 3) + (i & 7)) * (DM * 2));
    __builtin_amdgcn_sched_barrier(0);
    STAMP(1);
    lds_wait_ge(FL + F_H, 1);
    const unsigned tag = FL[F_TAG];
    const int ao = L_H + (n & 3) * H_STR + afo;
    gemv_group(R, *(const LAS v4u*)(L + ao), acc);
    __builtin_amdgcn_sched_barrier(0);
#pragma unroll
    for (int i = 0; i < 8; ++i) R[i] = ld16<NT>(rd, voff, (192 + i) * (DM * 2));
    __builtin_amdgcn_sched_barrier(0);
    gemv_group(R + 8, *(const LAS v4u*)(L + ao + 128), acc);
    __builtin_amdgcn_sched_barrier(0);
#pragma unroll
    for (int i = 0; i < 8; ++i) R[8 + i] = ld16<NT>(rd, voff4, (256 + i) * (DM * 2));
    __builtin_amdgcn_sched_barrier(0);
    gemv_group(R + 16, *(const LAS v4u*)(L + ao + 256), acc);
    gemv_group(R, *(const LAS v4u*)(L + ao + 384), acc);
    gemv_group(R + 8, *(const LAS v4u*)(L + ao + 512), acc);
    const f4 G = gemv_finish(acc, lane);
    const int rec = WS_F2 + (cs * 16 + rs) * 6144 + w * 2048 + lane * 16;
    st16_sc1(v4u{__float_as_uint(G[0]), tag, __float_as_uint(G[1]), tag}, rws, rec);
    st16_sc1(v4u{__float_as_uint(G[2]), tag, __float_as_uint(G[3]), tag}, rws, rec + 1024);
    STAMP(5);
  }
}

__global__ void knop(int* p){ if(p==nullptr) __builtin_trap(); }
extern "C" void kernel_launch(void* const* d_in, const int* in_sizes, int n_in, void* d_out, int out_size, void* d_ws,
                              size_t ws_size, hipStream_t stream) {
  fused_decode<<<NCU, NTHR, 0, stream>>>((const u16*)d_in[0], (const u16*)d_in[1], (const u16*)d_in[2], (const u16*)d_in[3],
                                         (const u16*)d_in[4], (const u16*)d_in[6], (const u16*)d_in[7], (const u16*)d_in[8],
                                         (u16*)d_out, (unsigned char*)d_ws + WS_BASE);
  knop<<<1,64,0,stream>>>((int*)d_ws);
}
```

```cpp
#include <hip/hip_runtime.h>
#include <stdint.h>

typedef unsigned short u16;
typedef unsigned v4u __attribute__((ext_vector_type(4)));
typedef unsigned v2u __attribute__((ext_vector_type(2)));
typedef float f4 __attribute__((ext_vector_type(4)));
typedef short s8v __attribute__((ext_vector_type(8)));
typedef __bf16 bf2v __attribute__((ext_vector_type(2)));
#define LAS __attribute__((address_space(3)))
#define GAS __attribute__((address_space(1)))
#define STR2(x) #x
#define STR(x) STR2(x)

constexpr int DM = 576, SK = 8192, WIN = 2048, FF = 4608, NQ = 4096;
constexpr int NCU = 256, NTHR = 768;
constexpr int N_S1 = 208, N_WO = 192, N_FIN = 144;
constexpr int OOB = 0x40000000;
constexpr int NT = 2, SC1 = 16;
constexpr float LOG2E = 1.4426950408889634f, QK_C = 0.08838834764831845f * LOG2E;

#define TAU 1000
#define AHEAD 24
#define AHEAD_D 24
#define VSPLIT 10
#define WOCNT 4
#define STAG 6
#define CODEPF 20480

constexpr int S1_ROW = (NQ + 2 * FF) * 4;
constexpr int WS_EPOCH = 0, WS_S1 = 4096, WS_H1 = WS_S1 + 4 * S1_ROW, WS_H2 = WS_H1 + NCU * 1024,
              WS_F2 = WS_H2 + 3 * 64 * 6144, WS_END = WS_F2 + 3 * 16 * 6144;
constexpr int XN_STR = 1168, H_STR = 656;
constexpr int L_XN = 0, L_H = L_XN + 4 * XN_STR, L_Q = L_H + 4 * H_STR, L_S1R = L_Q + 512, L_AM = L_S1R + 3 * 1024,
              L_AL = L_AM + 64, L_AD = L_AL + 64, L_AF = L_AD + 8 * 1024, L_XS = L_AF + 1024, L_DUM = L_XS + 5376,
              L_ZERO = L_DUM + 1024, L_FLAG = L_ZERO + 256, L_END = L_FLAG + 64;
enum { F_XN = 0, F_TAG, F_Q, F_H, F_S1, F_ISS, F_D, F_PUB, F_L, F_A, F_H2 };
static_assert(L_H % 16 == 0 && L_Q % 16 == 0 && L_S1R % 16 == 0 && L_AM % 16 == 0 && L_AD % 16 == 0 && L_XS % 16 == 0 &&
              L_DUM % 16 == 0 && L_ZERO % 16 == 0, "align");
#define GLB_N 65536
constexpr unsigned LDS_SPIN = 1u << 22, GLB_SPIN = GLB_N;

constexpr int WS_STAMP = 4 << 20;
#define STAMP(i) do {} while (0)
#define STAMP_FLUSH() do {} while (0)

#define PACE(kb) asm volatile("1:\n\ts_memrealtime s[92:93]\n\ts_waitcnt lgkmcnt(0)\n\ts_sub_u32 s92, s92, %0\n\ts_cmp_lt_i32 s92, %1\n\t" \
    "s_cbranch_scc0 2f\n\ts_sleep 1\n\ts_branch 1b\n2:" :: "s"(tbase), "i"((int)(((kb) * TAU) >> 8)) : "scc", "memory", "s92", "s93")
#define PLOAD(d, voff, soff) asm volatile("buffer_load_dwordx4 %0, %1, %2, %3 offen sc1" : "=v"(d) : "v"(voff), "s"(dws), "s"(soff) : "memory")
#define EXIT_WAVE { STAMP_FLUSH(); __builtin_amdgcn_endpgm(); __builtin_unreachable(); }

__device__ __forceinline__ __amdgpu_buffer_rsrc_t mkrsrc(const void* p, unsigned bytes) {
  return __builtin_amdgcn_make_buffer_rsrc((void*)p, 0, bytes, 0x00020000);
}
__device__ __forceinline__ v4u ld_nt(__amdgpu_buffer_rsrc_t r, int voff, int soff = 0) {
  return __builtin_amdgcn_raw_buffer_load_b128(r, voff, soff, NT);
}
__device__ __forceinline__ void st_sc1(v4u v, __amdgpu_buffer_rsrc_t r, int voff) {
  __builtin_amdgcn_raw_buffer_store_b128(v, r, voff, 0, SC1);
}
__device__ __forceinline__ f4 mfma(v4u a, v4u b, f4 c) {
  return __builtin_amdgcn_mfma_f32_16x16x32_bf16(__builtin_bit_cast(s8v, a), __builtin_bit_cast(s8v, b), c, 0, 0, 0);
}
__device__ __forceinline__ unsigned cvtpk(float lo, float hi) {
  bf2v r = {(__bf16)lo, (__bf16)hi};
  return __builtin_bit_cast(unsigned, r);
}
__device__ __forceinline__ float rbf(float f) { return (float)(__bf16)f; }
__device__ __forceinline__ float rcp(float f) { return __builtin_amdgcn_rcpf(f); }
__device__ __forceinline__ float ex2(float f) { return __builtin_amdgcn_exp2f(f); }
__device__ __forceinline__ float blo(unsigned u) { return __uint_as_float(u << 16); }
__device__ __forceinline__ float bhi(unsigned u) { return __uint_as_float(u & 0xffff0000u); }
__device__ __forceinline__ unsigned mktag(unsigned e) { return ((e + 1u) & 0xffffu) ? e + 1u : e + 2u; }
template <int CTRL> __device__ __forceinline__ float dpp(float x) {
  return __builtin_bit_cast(float, __builtin_amdgcn_mov_dpp(__builtin_bit_cast(int, x), CTRL, 0xf, 0xf, true));
}
constexpr int ROR1 = 0x121, ROR2 = 0x122, ROR4 = 0x124, ROR8 = 0x128, XOR1 = 0xB1;
__device__ __forceinline__ float row_sum(float x) { x += dpp<ROR8>(x); x += dpp<ROR4>(x); x += dpp<ROR2>(x); x += dpp<ROR1>(x); return x; }
__device__ __forceinline__ float row_max(float x) {
  x = fmaxf(x, dpp<ROR8>(x)); x = fmaxf(x, dpp<ROR4>(x)); x = fmaxf(x, dpp<ROR2>(x)); x = fmaxf(x, dpp<ROR1>(x)); return x; }
__device__ __forceinline__ void swap16(float& a, float& b) {
  auto s = __builtin_amdgcn_permlane16_swap(__float_as_uint(a), __float_as_uint(b), false, false);
  a = __uint_as_float(s[0]); b = __uint_as_float(s[1]);
}
__device__ __forceinline__ void swap32(float& a, float& b) {
  auto s = __builtin_amdgcn_permlane32_swap(__float_as_uint(a), __float_as_uint(b), false, false);
  a = __uint_as_float(s[0]); b = __uint_as_float(s[1]);
}
__device__ __forceinline__ float xrow_sum(float x) { float a = x, b = x; swap16(a, b); x = a + b; a = x; b = x; swap32(a, b); return a + b; }
__device__ __forceinline__ float xrow_max(float x) { float a = x, b = x; swap16(a, b); x = fmaxf(a, b); a = x; b = x; swap32(a, b); return fmaxf(a, b); }
__device__ __forceinline__ float pair_sum(float a, float b) {
  a += dpp<ROR8>(a); b += dpp<ROR8>(b);
  swap16(a, b);
  float t = a + b, u = t;
  swap32(t, u);
  return t + u;
}

template <int SLEEP> __device__ __forceinline__ void lds_wait(volatile LAS unsigned* f, unsigned want) {
#pragma nounroll
  for (unsigned s = 0; s < LDS_SPIN; ++s) {
    if ((unsigned)__builtin_amdgcn_readfirstlane(*f) >= want) break;
    if (SLEEP) __builtin_amdgcn_s_sleep(SLEEP);
  }
  asm volatile("" ::: "memory");
}
__device__ __forceinline__ void lds_post(int flag) {
  asm volatile("s_mov_b64 exec, 1\n\tds_add_u32 %0, %1\n\ts_mov_b64 exec, -1" :: "v"(L_FLAG + 4 * flag), "v"(1u) : "memory");
}
__device__ __forceinline__ unsigned lds_arrive(int flag) {
  unsigned o, r;
  asm volatile("s_mov_b64 exec, 1\n\tds_add_rtn_u32 %0, %2, %3\n\ts_mov_b64 exec, -1\n\ts_waitcnt lgkmcnt(0)\n\tv_readfirstlane_b32 %1, %0"
               : "=&v"(o), "=s"(r) : "v"(L_FLAG + 4 * flag), "v"(1u) : "memory");
  return r;
}

__device__ __forceinline__ void gemv_group(const v4u* r, v4u a, f4* acc) {
#pragma unroll
  for (int w = 0; w < 4; ++w) {
    v4u lo, hi;
#pragma unroll
    for (int p = 0; p < 4; ++p) {
      lo[p] = __builtin_amdgcn_perm(r[2 * p + 1][w], r[2 * p][w], 0x05040100u);
      hi[p] = __builtin_amdgcn_perm(r[2 * p + 1][w], r[2 * p][w], 0x07060302u);
    }
    acc[2 * w] = mfma(a, lo, acc[2 * w]);
    acc[2 * w + 1] = mfma(a, hi, acc[2 * w + 1]);
  }
}
__device__ __forceinline__ f4 gemv_fold(const f4* e, int lane) {
  f4 G;
#pragma unroll
  for (int j = 0; j < 4; ++j) {
    float e0 = e[j][0], e1 = e[j][1], e2 = e[j][2], e3 = e[j][3];
    swap16(e0, e1); swap16(e2, e3);
    const float t0 = e0 + dpp<ROR8>(e1), t1 = dpp<ROR8>(e2) + e3;
    G[j] = (lane & 8) ? t1 : t0;
  }
  return G;
}
__device__ __forceinline__ f4 gemv_finish(const f4* acc, int lane) {
  f4 e[4];
#pragma unroll
  for (int j = 0; j < 4; ++j) e[j] = lane < 32 ? acc[j] : acc[j + 4];
  return gemv_fold(e, lane);
}

__global__ void __launch_bounds__(NTHR, 3)
fused_decode(const u16* __restrict__ x, const u16* __restrict__ kc, const u16* __restrict__ vc, const u16* __restrict__ wq,
             const u16* __restrict__ wo, const u16* __restrict__ wg, const u16* __restrict__ wu, const u16* __restrict__ wd,
             u16* __restrict__ out, unsigned char* ws) {
  __shared__ __attribute__((aligned(16))) unsigned char lds_raw[L_END];
  LAS unsigned char* L = (LAS unsigned char*)lds_raw;
  volatile LAS unsigned* FL = (volatile LAS unsigned*)(L + L_FLAG);
  const unsigned tstart = (unsigned)__builtin_amdgcn_s_memrealtime();
  if (threadIdx.x < 80) *(volatile LAS unsigned*)(L + L_ZERO + threadIdx.x * 4) = 0u;
  asm volatile("s_waitcnt lgkmcnt(0)\n\ts_barrier" ::: "memory");
  asm volatile("" :: "s"(x), "s"(kc), "s"(vc), "s"(wq), "s"(wo), "s"(wg), "s"(wu), "s"(wd), "s"(out), "s"(ws));
  const int c = blockIdx.x, wave = __builtin_amdgcn_readfirstlane(threadIdx.x >> 6);
#define LANE_IDS int lane_ = threadIdx.x & 63; asm volatile("" : "+v"(lane_)); const int lane = lane_, g = lane >> 4, n = lane & 15;
  const int pair = (c & 7) * 8 + (c >> 5), sp = (c >> 3) & 3;
  const int ksl = (c & 7) * 8 + (c >> 3) / 3, cs = (c >> 3) % 3;
  const __amdgpu_buffer_rsrc_t rws = mkrsrc(ws, WS_END);
  if (wave == 3) {
    const v4u dx = {(unsigned)(uintptr_t)x, (unsigned)((uintptr_t)x >> 32), 4u * DM * 2u, 0x00020000u};
    const v4u de = {(unsigned)(uintptr_t)ws, (unsigned)((uintptr_t)ws >> 32), 4u, 0x00020000u};
    const int xo = (threadIdx.x & 63) * 16, z = 0;
    unsigned keep;
    asm volatile("s_mov_b32 %0, m0\n\ts_mov_b32 m0, %4\n\ts_nop 0\n\t"
                 "buffer_load_dwordx4 %1, %2, 0 offen lds\n\tbuffer_load_dwordx4 %1, %2, 0 offen offset:1024 lds\n\t"
                 "buffer_load_dwordx4 %1, %2, 0 offen offset:2048 lds\n\tbuffer_load_dwordx4 %1, %2, 0 offen offset:3072 lds\n\t"
                 "s_mov_b32 m0, %5\n\ts_movk_i32 s92, 0x1000\n\tbuffer_load_dwordx4 %1, %2, s92 offen lds\n\t"
                 "s_mov_b32 m0, %6\n\ts_nop 0\n\tbuffer_load_dword %7, %3, 0 offen sc1 lds\n\ts_mov_b32 m0, %0"
                 : "=&s"(keep) : "v"(xo), "s"(dx), "s"(de), "i"(L_XS), "i"(L_XS + 4096), "i"(L_XS + 5120), "v"(z) : "memory", "s92");
  }
  if (wave == 11) {
    const int po = (c >> 3) * 1024 + (threadIdx.x & 63) * 16;
    unsigned keep;
    asm volatile("s_mov_b32 %0, m0\n\ts_getpc_b64 s[92:93]\n\ts_and_b32 s92, s92, 0xffffff00\n\ts_mov_b32 s94, %2\n\ts_mov_b32 s95, 0x20000\n\t"
                 "s_mov_b32 m0, %3\n\ts_nop 0\n\tbuffer_load_dwordx4 %1, s[92:95], 0 offen lds\n\ts_mov_b32 m0, %0"
                 : "=&s"(keep) : "v"(po), "i"(CODEPF), "i"(L_DUM) : "memory", "scc", "s92", "s93", "s94", "s95");
  }

  if (__builtin_expect(wave >= 4, 0)) {
    LANE_IDS
    const int w = wave - 4;
    const size_t kvo = ((size_t)pair * SK + (SK - WIN) + sp * 512 + ((w + pair) & 7) * 64) * 128;
    const __amdgpu_buffer_rsrc_t rk = mkrsrc(kc + kvo, 16384), rv = mkrsrc(vc + kvo, 16384);
    const int kvo0 = (n >> 2) * 4096 + (n >> 1) * 256 + (n & 1) * 64 + g * 16, vvo0 = g * 4096 + n * 16;
    const unsigned tbase = tstart + (unsigned)((((c < N_S1 ? 72 - AHEAD : -AHEAD_D) + 2 * w) * TAU) >> 8);
    v4u K[16], V[16];
#pragma unroll
    for (int i = 0; i < 16; ++i) {
      if ((i & 1) == 0) PACE(8 * i);
      K[i] = ld_nt(rk, (kvo0 ^ ((i >> 2) * 4096)) + ((i >> 1) & 1) * 2048 + (i & 1) * 128);
    }
#define V_LOAD(i) { if (((i) & 1) == 0) PACE(128 + 8 * (i)); V[i] = ld_nt(rv, (vvo0 ^ (((i) >> 2) * 4096)) + (((i) & 1) * 8 + ((i) >> 1)) * 256); }
#pragma unroll
    for (int i = 0; i < VSPLIT; ++i) V_LOAD(i)
    __builtin_amdgcn_sched_barrier(0);
    STAMP(2);
    float m0, m1; unsigned pk;
    auto scores = [&]() __attribute__((always_inline)) {
      f4 E0 = f4{0.f, 0.f, 0.f, 0.f}, E1 = f4{0.f, 0.f, 0.f, 0.f};
      const int qa = L_Q + ((n >> 1) & 1) * 256 + (n & 1) * 64 + g * 16;
#pragma unroll
      for (int tp = 0; tp < 4; ++tp) {
        const int ad = (n >> 2) == tp ? qa : L_ZERO;
        const v4u q0 = *(const LAS v4u*)(L + ad), q1 = *(const LAS v4u*)(L + ad + 128);
        E0 = mfma(q0, K[4 * tp], E0); E1 = mfma(q0, K[4 * tp + 2], E1);
        E0 = mfma(q1, K[4 * tp + 1], E0); E1 = mfma(q1, K[4 * tp + 3], E1);
      }
      const float s0 = ((n & 1) ? E1[1] : E0[0]) + dpp<XOR1>((n & 1) ? E0[1] : E1[0]);
      const float s1 = ((n & 1) ? E1[3] : E0[2]) + dpp<XOR1>((n & 1) ? E0[3] : E1[2]);
      m0 = xrow_max(row_max(s0)); m1 = xrow_max(row_max(s1));
      const float p0 = ex2((s0 - m0) * QK_C), p1 = ex2((s1 - m1) * QK_C);
      const float l0 = xrow_sum(row_sum(p0)), l1 = xrow_sum(row_sum(p1));
      if (lane == 0) {
        *(LAS float*)(L + L_AM + w * 4) = m0; *(LAS float*)(L + L_AM + 32 + w * 4) = m1;
        *(LAS float*)(L + L_AL + w * 4) = l0; *(LAS float*)(L + L_AL + 32 + w * 4) = l1;
      }
      lds_post(F_L);
      pk = cvtpk(p0, p1);
    };
    const bool early = __builtin_amdgcn_readfirstlane(FL[F_Q]) != 0;
    asm volatile("" ::: "memory");
    if (early) scores();
    STAMP(3);
    __builtin_amdgcn_sched_barrier(0);
#pragma unroll
    for (int i = VSPLIT; i < 16; ++i) V_LOAD(i)
    __builtin_amdgcn_sched_barrier(0);
    lds_post(F_ISS);
    STAMP(1);
    if (!early) { lds_wait<1>(FL + F_Q, 1); scores(); }
    lds_wait<1>(FL + F_L, 8);
    float scale; unsigned t1, t3;
    {
      const int h = g >> 1;
      const f4 ma = *(const LAS f4*)(L + L_AM + h * 32), mb = *(const LAS f4*)(L + L_AM + h * 32 + 16);
      const f4 la = *(const LAS f4*)(L + L_AL + h * 32), lb = *(const LAS f4*)(L + L_AL + h * 32 + 16);
      const float Mh = fmaxf(fmaxf(fmaxf(ma[0], ma[1]), fmaxf(ma[2], ma[3])), fmaxf(fmaxf(mb[0], mb[1]), fmaxf(mb[2], mb[3])));
      float Lh = 0.f;
#pragma unroll
      for (int k = 0; k < 4; ++k) Lh = fmaf(la[k], ex2((ma[k] - Mh) * QK_C), Lh);
#pragma unroll
      for (int k = 0; k < 4; ++k) Lh = fmaf(lb[k], ex2((mb[k] - Mh) * QK_C), Lh);
      scale = ex2(((h ? m1 : m0) - Mh) * QK_C) * rcp(Lh);
      const unsigned th = FL[F_TAG] << 16, ml = __float_as_uint((g & 1) ? Lh : Mh);
      t1 = th | (ml & 0xffffu); t3 = th | (ml >> 16);
    }
    int oh = WS_H1 + (pair * 4 + sp) * 1024 + (g >> 1) * 512 + n * 32 + (g & 1) * 16, la_ = L_AD + lane * 16;
    const unsigned hs = (n & 8) ? 0x0302u : 0x0100u, one = (n & 1) ? ((hs << 16) | 0x0c0cu) : (0x0c0c0000u | hs);
    unsigned sel[4];
#pragma unroll
    for (int r = 0; r < 4; ++r) sel[r] = ((n & 7) >> 1) == r ? one : 0x0c0c0c0cu;
    asm volatile("" : "+v"(scale), "+v"(t1), "+v"(t3), "+v"(oh), "+v"(la_));
    __builtin_amdgcn_sched_barrier(0);
    f4 O = f4{0.f, 0.f, 0.f, 0.f};
#define PV_STEP(i) { const unsigned pb = (unsigned)__builtin_amdgcn_mov_dpp((int)pk, 0x150 + (i), 0xf, 0xf, true); \
      v4u a; a[0] = __builtin_amdgcn_perm(pb, pb, sel[0]); a[1] = __builtin_amdgcn_perm(pb, pb, sel[1]); \
      a[2] = __builtin_amdgcn_perm(pb, pb, sel[2]); a[3] = __builtin_amdgcn_perm(pb, pb, sel[3]); O = mfma(a, V[i], O); }
    PV_STEP(0) PV_STEP(1) PV_STEP(2) PV_STEP(3) PV_STEP(4) PV_STEP(5) PV_STEP(6) PV_STEP(7)
    PV_STEP(8) PV_STEP(9) PV_STEP(10) PV_STEP(11) PV_STEP(12) PV_STEP(13) PV_STEP(14) PV_STEP(15)
    *(LAS f4*)(L + la_ + w * 1024) = O * scale;
    STAMP(4);
    if (lds_arrive(F_D) == 7) {
      f4 t[8];
#pragma unroll
      for (int k = 0; k < 8; ++k) t[k] = *(const LAS f4*)(L + la_ + k * 1024);
      const f4 o = ((t[0] + t[1]) + (t[2] + t[3])) + ((t[4] + t[5]) + (t[6] + t[7]));
      st_sc1(v4u{cvtpk(o[0], o[1]), t1, cvtpk(o[2], o[3]), t3}, rws, oh);
      FL[F_PUB] = 1;
      STAMP(5);
    }
    EXIT_WAVE
  }

  if (__builtin_expect(wave == 3, 0)) {
    __builtin_amdgcn_s_setprio(3);
    LANE_IDS
    const v4u dws = {(unsigned)(uintptr_t)ws, (unsigned)((uintptr_t)ws >> 32), (unsigned)WS_END, 0x00020000u};
    const int zero = 0;
    asm volatile("s_waitcnt vmcnt(0)" ::: "memory");
    const unsigned tag = mktag(*(const volatile LAS unsigned*)(L + L_XS + 5120));
    if (c < N_S1) {
      float xf[40], s = 0.f;
#pragma unroll
      for (int i = 0; i < 5; ++i) {
        const v4u X = *(const volatile LAS v4u*)(L + ((i == 4 && n >= 8) ? L_ZERO : L_XS + g * (DM * 2) + (n + 16 * i) * 16));
#pragma unroll
        for (int k = 0; k < 4; ++k) { xf[i * 8 + 2 * k] = blo(X[k]); xf[i * 8 + 2 * k + 1] = bhi(X[k]); }
      }
#pragma unroll
      for (int i = 0; i < 40; ++i) s += xf[i];
      const float mean = row_sum(s) * (1.f / DM);
      float v = 0.f;
#pragma unroll
      for (int i = 0; i < 40; ++i) { const float d = (i >= 32 && n >= 8) ? 0.f : xf[i] - mean; v = fmaf(d, d, v); }
      const float var = row_sum(v) * (1.f / DM);
      const float mb = rbf(mean), rden = rcp(rbf(__builtin_amdgcn_sqrtf(rbf(rbf(var) + 1e-5f))));
#pragma unroll
      for (int i = 0; i < 5; ++i) {
        v4u o;
#pragma unroll
        for (int k = 0; k < 4; ++k) o[k] = cvtpk(rbf(xf[i * 8 + 2 * k] - mb) * rden, rbf(xf[i * 8 + 2 * k + 1] - mb) * rden);
        if (i < 4 || n < 8) *(LAS v4u*)(L + L_XN + g * XN_STR + (n + 16 * i) * 16) = o;
      }
    }
    FL[F_TAG] = tag;
    asm volatile("" ::: "memory");
    FL[F_XN] = 1;
    STAMP(1);
    {
      const int off = WS_S1 + (pair >> 4) * S1_ROW + (pair & 15) * 1024 + lane * 16;
      v4u r;
#pragma nounroll
      for (unsigned s = 0; s < GLB_SPIN; ++s) {
        PLOAD(r, off, zero);
        asm volatile("s_waitcnt vmcnt(0)" : "+v"(r) :: "memory");
        if (__all(r[1] == tag && r[3] == tag)) break;
      }
      *(LAS v2u*)(L + L_Q + lane * 8) = v2u{r[0], r[2]};
      asm volatile("" ::: "memory");
      FL[F_Q] = 1;
    }
    STAMP(2);
    if (c >= N_S1) {
      const int rs = (c - N_S1) / 3, up = FF * 4;
      v4u G[5], U[5];
      int off[5], loff[5];
#pragma unroll
      for (int i = 0; i < 5; ++i) {
        const int t = min(lane + 64 * i, 287), b = t / 72, qd = t - b * 72;
        off[i] = WS_S1 + b * S1_ROW + (NQ + 288 * rs + 4 * qd) * 4; loff[i] = b * H_STR + qd * 8;
      }
#pragma nounroll
      for (unsigned s = 0; s < GLB_SPIN; ++s) {
#pragma unroll
        for (int i = 0; i < 5; ++i) { PLOAD(G[i], off[i], zero); PLOAD(U[i], off[i], up); }
        asm volatile("s_waitcnt vmcnt(0)" : "+v"(G[0]), "+v"(G[1]), "+v"(G[2]), "+v"(G[3]), "+v"(G[4]),
                     "+v"(U[0]), "+v"(U[1]), "+v"(U[2]), "+v"(U[3]), "+v"(U[4]) :: "memory");
        bool ok = true;
#pragma unroll
        for (int i = 0; i < 5; ++i) ok &= (G[i][1] == tag) & (G[i][3] == tag) & (U[i][1] == tag) & (U[i][3] == tag);
        if (__all(ok)) break;
        __builtin_amdgcn_s_sleep(4);
      }
      if (lane < 32) *(LAS v2u*)(L + L_H + (lane >> 3) * H_STR + 576 + (lane & 7) * 8) = v2u{0u, 0u};
#pragma unroll
      for (int i = 0; i < 5; ++i) {
        float hv[4];
#pragma unroll
        for (int k = 0; k < 4; ++k) {
          const unsigned gw = G[i][(k >> 1) * 2], uw = U[i][(k >> 1) * 2];
          const float a = (k & 1) ? bhi(gw) : blo(gw), u = (k & 1) ? bhi(uw) : blo(uw);
          const float z2 = (-2.f * 0.7978845608028654f * LOG2E) * (a + 0.044715f * a * a * a);
          hv[k] = rbf(a * rcp(1.f + ex2(z2))) * u;
        }
        *(LAS v2u*)(L + L_H + loff[i]) = v2u{cvtpk(hv[0], hv[1]), cvtpk(hv[2], hv[3])};
      }
      asm volatile("" ::: "memory");
      FL[F_H] = 1;
      STAMP(3);
    }
    if (c >= N_WO) EXIT_WAVE
    const int fcs = c / 48, fr = c - fcs * 48, fw = fr >> 4, fpc = (fr >> 3) & 1, fgr = fr & 7;
    int oo = ((2 * (fgr & 1) + ((fgr >> 1) & 1)) * DM + 64 * (3 * fcs + fw) + 8 * (lane & 7) + 4 * (fgr >> 2) + 2 * fpc + (g & 1)) * 2;
    oo = (c < N_FIN && (lane & 40) == 0) ? oo : OOB;
    float xres = blo(__builtin_amdgcn_raw_buffer_load_b16(mkrsrc(x, 4 * DM * 2), oo, 0, 0));
    asm volatile("" : "+v"(xres), "+v"(oo));
    {
      const int hh = ksl >> 1, pc = (((n & 3) * 16 + (hh >> 1)) * 4 + 2 * (n >> 3));
      const int o1 = WS_H1 + pc * 1024 + (hh & 1) * 512 + (8 * (ksl & 1) + 4 * ((n >> 2) & 1) + g) * 32;
      unsigned p[12];
#pragma unroll
      for (int k = 0; k < 12; ++k) p[k] = 0u;
      lds_wait<1>(FL + F_PUB, 1);
      STAMP(3);
      asm volatile(
        "s_mov_b64 s[70:71], 0\n\t" "s_mov_b32 s72, %[n]\n\t"
        "v_cndmask_b32_e64 v119, %[o1], 2.0, s[70:71]\n\t"
        "buffer_load_dwordx4 v[120:123], v119, %[d], 0 offen offset:0 sc1\n\t"
        "buffer_load_dwordx4 v[124:127], v119, %[d], 0 offen offset:16 sc1\n\t"
        "buffer_load_dwordx4 v[128:131], v119, %[d], 0 offen offset:1024 sc1\n\t"
        "buffer_load_dwordx4 v[132:135], v119, %[d], 0 offen offset:1040 sc1\n\t"
        "s_sleep " STR(STAG) "\n\t"
        "1:\n\t"
        "v_cndmask_b32_e64 v119, %[o1], 2.0, s[70:71]\n\t"
        "buffer_load_dwordx4 v[136:139], v119, %[d], 0 offen offset:0 sc1\n\t"
        "buffer_load_dwordx4 v[140:143], v119, %[d], 0 offen offset:16 sc1\n\t"
        "buffer_load_dwordx4 v[144:147], v119, %[d], 0 offen offset:1024 sc1\n\t"
        "buffer_load_dwordx4 v[148:151], v119, %[d], 0 offen offset:1040 sc1\n\t"
        "s_waitcnt vmcnt(4)\n\t"
        "v_bitop3_b32 v112, v121, v123, %[t] bitop3:0x7e\n\t"
        "v_bitop3_b32 v113, v125, v127, %[t] bitop3:0x7e\n\t"
        "v_bitop3_b32 v114, v129, v131, %[t] bitop3:0x7e\n\t"
        "v_bitop3_b32 v115, v133, v135, %[t] bitop3:0x7e\n\t"
        "v_or3_b32 v112, v112, v113, v114\n\t" "v_or_b32 v112, v112, v115\n\t" "v_cmp_gt_u32 vcc, 0x10000, v112\n\t"
        "v_perm_b32 v113, v123, v121, %[sl]\n\t"
        "v_perm_b32 v114, v127, v125, %[sl]\n\t"
        "v_perm_b32 v115, v131, v129, %[sl]\n\t"
        "v_perm_b32 v116, v135, v133, %[sl]\n\t"
        "s_or_b64 s[70:71], s[70:71], vcc\n\t"
        "v_cndmask_b32 %[p0], %[p0], v120, vcc\n\t"
        "v_cndmask_b32 %[p1], %[p1], v122, vcc\n\t"
        "v_cndmask_b32 %[p2], %[p2], v124, vcc\n\t"
        "v_cndmask_b32 %[p3], %[p3], v126, vcc\n\t"
        "v_cndmask_b32 %[p4], %[p4], v128, vcc\n\t"
        "v_cndmask_b32 %[p5], %[p5], v130, vcc\n\t"
        "v_cndmask_b32 %[p6], %[p6], v132, vcc\n\t"
        "v_cndmask_b32 %[p7], %[p7], v134, vcc\n\t"
        "v_cndmask_b32 %[p8], %[p8], v113, vcc\n\t"
        "v_cndmask_b32 %[p9], %[p9], v114, vcc\n\t"
        "v_cndmask_b32 %[p10], %[p10], v115, vcc\n\t"
        "v_cndmask_b32 %[p11], %[p11], v116, vcc\n\t"
        "s_cmp_eq_u64 s[70:71], exec\n\t" "s_cbranch_scc1 2f\n\t"
        "v_cndmask_b32_e64 v119, %[o1], 2.0, s[70:71]\n\t"
        "buffer_load_dwordx4 v[120:123], v119, %[d], 0 offen offset:0 sc1\n\t"
        "buffer_load_dwordx4 v[124:127], v119, %[d], 0 offen offset:16 sc1\n\t"
        "buffer_load_dwordx4 v[128:131], v119, %[d], 0 offen offset:1024 sc1\n\t"
        "buffer_load_dwordx4 v[132:135], v119, %[d], 0 offen offset:1040 sc1\n\t"
        "s_waitcnt vmcnt(4)\n\t"
        "v_bitop3_b32 v112, v137, v139, %[t] bitop3:0x7e\n\t"
        "v_bitop3_b32 v113, v141, v143, %[t] bitop3:0x7e\n\t"
        "v_bitop3_b32 v114, v145, v147, %[t] bitop3:0x7e\n\t"
        "v_bitop3_b32 v115, v149, v151, %[t] bitop3:0x7e\n\t"
        "v_or3_b32 v112, v112, v113, v114\n\t" "v_or_b32 v112, v112, v115\n\t" "v_cmp_gt_u32 vcc, 0x10000, v112\n\t"
        "v_perm_b32 v113, v139, v137, %[sl]\n\t"
        "v_perm_b32 v114, v143, v141, %[sl]\n\t"
        "v_perm_b32 v115, v147, v145, %[sl]\n\t"
        "v_perm_b32 v116, v151, v149, %[sl]\n\t"
        "s_or_b64 s[70:71], s[70:71], vcc\n\t"
        "v_cndmask_b32 %[p0], %[p0], v136, vcc\n\t"
        "v_cndmask_b32 %[p1], %[p1], v138, vcc\n\t"
        "v_cndmask_b32 %[p2], %[p2], v140, vcc\n\t"
        "v_cndmask_b32 %[p3], %[p3], v142, vcc\n\t"
        "v_cndmask_b32 %[p4], %[p4], v144, vcc\n\t"
        "v_cndmask_b32 %[p5], %[p5], v146, vcc\n\t"
        "v_cndmask_b32 %[p6], %[p6], v148, vcc\n\t"
        "v_cndmask_b32 %[p7], %[p7], v150, vcc\n\t"
        "v_cndmask_b32 %[p8], %[p8], v113, vcc\n\t"
        "v_cndmask_b32 %[p9], %[p9], v114, vcc\n\t"
        "v_cndmask_b32 %[p10], %[p10], v115, vcc\n\t"
        "v_cndmask_b32 %[p11], %[p11], v116, vcc\n\t"
        "s_cmp_eq_u64 s[70:71], exec\n\t" "s_cbranch_scc1 2f\n\t"
        "s_bcnt0_i32_b64 s73, s[70:71]\n\t" "s_cmp_lt_u32 s73, 17\n\t" "s_cbranch_scc1 3f\n\t" "s_sleep 12\n\t" "3:\n\t"
        "s_sub_u32 s72, s72, 1\n\t" "s_cbranch_scc0 1b\n\t"
        "2:\n\t" "s_nop 1 ; POLL_END"
        : [p0] "+v"(p[0]), [p1] "+v"(p[1]), [p2] "+v"(p[2]), [p3] "+v"(p[3]), [p4] "+v"(p[4]), [p5] "+v"(p[5]), [p6] "+v"(p[6]), [p7] "+v"(p[7]), [p8] "+v"(p[8]), [p9] "+v"(p[9]), [p10] "+v"(p[10]), [p11] "+v"(p[11])
        : [o1] "v"(o1), [d] "s"(dws), [t] "v"(tag << 16), [sl] "s"(0x05040100u), [n] "i"(GLB_SPIN)
        : "memory", "vcc", "scc", "s70", "s71", "s72", "s73", "v112", "v113", "v114", "v115", "v116", "v117", "v118", "v119", "v120", "v121", "v122", "v123", "v124", "v125", "v126", "v127", "v128", "v129", "v130", "v131", "v132", "v133", "v134", "v135", "v136", "v137", "v138", "v139", "v140", "v141", "v142", "v143", "v144", "v145", "v146", "v147", "v148", "v149", "v150", "v151");
      const float ma = __uint_as_float(p[8]), mb = __uint_as_float(p[10]);
      float M = fmaxf(ma, mb); M = fmaxf(M, dpp<ROR8>(M));
      const float ea = __uint_as_float(p[9]) * ex2((ma - M) * QK_C), eb = __uint_as_float(p[11]) * ex2((mb - M) * QK_C);
      float Ls = ea + eb; Ls += dpp<ROR8>(Ls);
      const float inv = rcp(Ls);
      v4u a;
#pragma unroll
      for (int k = 0; k < 4; ++k) {
        float lo = fmaf(ea, blo(p[k]), eb * blo(p[4 + k])), hi = fmaf(ea, bhi(p[k]), eb * bhi(p[4 + k]));
        lo += dpp<ROR8>(lo); hi += dpp<ROR8>(hi);
        a[k] = cvtpk(lo * inv, hi * inv);
      }
      *(LAS v4u*)(L + L_AF + lane * 16) = a;
      asm volatile("" ::: "memory");
      FL[F_A] = 1;
      STAMP(4);
      asm volatile("s_waitcnt vmcnt(0) ; POLL_DRAIN" ::: "memory");
    }
    if (c >= N_FIN) EXIT_WAVE
    const int lo_ = fw * 2048 + fpc * 1024 + fgr * 128 + (lane & 7) * 16;
    int o[9]; float pa[8], pb[8], f0 = 0.f, f1 = 0.f;
    o[8] = WS_F2 + (fcs * 16 + (lane >> 3)) * 6144 + lo_;
#pragma unroll
    for (int i = 0; i < 8; ++i) { o[i] = WS_H2 + (fcs * 64 + 8 * i + (lane >> 3)) * 6144 + lo_; pa[i] = 0.f; pb[i] = 0.f; }
    const __amdgpu_buffer_rsrc_t rout = mkrsrc(out, 4 * DM * 2);
    int oe = (c | lane) ? OOB : WS_EPOCH;
    asm volatile("" : "+v"(oe));
    lds_wait<0>(FL + F_H2, 3);
    asm volatile(
        "s_mov_b32 s72, " STR(GLB_N) "\n\t" "s_mov_b32 s73, 0xc000\n\t"
        "buffer_load_dwordx4 v[88:91], %[o0], %[d], 0 offen sc1\n\t"
        "buffer_load_dwordx4 v[92:95], %[o1], %[d], 0 offen sc1\n\t"
        "buffer_load_dwordx4 v[96:99], %[o2], %[d], 0 offen sc1\n\t"
        "buffer_load_dwordx4 v[100:103], %[o3], %[d], 0 offen sc1\n\t"
        "buffer_load_dwordx4 v[104:107], %[o4], %[d], 0 offen sc1\n\t"
        "buffer_load_dwordx4 v[108:111], %[o5], %[d], 0 offen sc1\n\t"
        "buffer_load_dwordx4 v[112:115], %[o6], %[d], 0 offen sc1\n\t"
        "buffer_load_dwordx4 v[116:119], %[o7], %[d], 0 offen sc1\n\t"
        "buffer_load_dwordx4 v[120:123], %[o8], %[d], 0 offen sc1\n\t"
        "buffer_load_dwordx4 v[124:127], %[o8], %[d], s73 offen sc1\n\t"
        "s_sleep " STR(STAG) "\n\t"
        "1:\n\t"
        "buffer_load_dwordx4 v[128:131], %[o0], %[d], 0 offen sc1\n\t"
        "buffer_load_dwordx4 v[132:135], %[o1], %[d], 0 offen sc1\n\t"
        "buffer_load_dwordx4 v[136:139], %[o2], %[d], 0 offen sc1\n\t"
        "buffer_load_dwordx4 v[140:143], %[o3], %[d], 0 offen sc1\n\t"
        "buffer_load_dwordx4 v[144:147], %[o4], %[d], 0 offen sc1\n\t"
        "buffer_load_dwordx4 v[148:151], %[o5], %[d], 0 offen sc1\n\t"
        "buffer_load_dwordx4 v[152:155], %[o6], %[d], 0 offen sc1\n\t"
        "buffer_load_dwordx4 v[156:159], %[o7], %[d], 0 offen sc1\n\t"
        "buffer_load_dwordx4 v[160:163], %[o8], %[d], 0 offen sc1\n\t"
        "buffer_load_dwordx4 v[164:167], %[o8], %[d], s73 offen sc1\n\t"
        "s_waitcnt vmcnt(10)\n\t"
        "v_bitop3_b32 v78, v89, v91, %[t] bitop3:0x7e\n\t"
        "v_bitop3_b32 v79, v93, v95, %[t] bitop3:0x7e\n\t"
        "v_bitop3_b32 v80, v97, v99, %[t] bitop3:0x7e\n\t"
        "v_bitop3_b32 v81, v101, v103, %[t] bitop3:0x7e\n\t"
        "v_bitop3_b32 v82, v105, v107, %[t] bitop3:0x7e\n\t"
        "v_bitop3_b32 v83, v109, v111, %[t] bitop3:0x7e\n\t"
        "v_bitop3_b32 v84, v113, v115, %[t] bitop3:0x7e\n\t"
        "v_bitop3_b32 v85, v117, v119, %[t] bitop3:0x7e\n\t"
        "v_bitop3_b32 v86, v121, v123, %[t] bitop3:0x7e\n\t"
        "v_bitop3_b32 v87, v125, v127, %[t] bitop3:0x7e\n\t"
        "v_or_b32 v86, v86, v87\n\t"
        "v_cmp_eq_u32 s[74:75], 0, v78\n\t"
        "v_cmp_eq_u32 s[76:77], 0, v79\n\t"
        "v_cmp_eq_u32 s[78:79], 0, v80\n\t"
        "v_cmp_eq_u32 s[80:81], 0, v81\n\t"
        "v_cmp_eq_u32 s[82:83], 0, v82\n\t"
        "v_cmp_eq_u32 s[84:85], 0, v83\n\t"
        "v_cmp_eq_u32 s[86:87], 0, v84\n\t"
        "v_cmp_eq_u32 s[88:89], 0, v85\n\t"
        "v_cmp_eq_u32 s[90:91], 0, v86\n\t"
        "v_cndmask_b32_e64 %[o0], %[o0], 2.0, s[74:75]\n\t"
        "v_cndmask_b32_e64 %[o1], %[o1], 2.0, s[76:77]\n\t"
        "v_cndmask_b32_e64 %[o2], %[o2], 2.0, s[78:79]\n\t"
        "v_cndmask_b32_e64 %[o3], %[o3], 2.0, s[80:81]\n\t"
        "v_cndmask_b32_e64 %[o4], %[o4], 2.0, s[82:83]\n\t"
        "v_cndmask_b32_e64 %[o5], %[o5], 2.0, s[84:85]\n\t"
        "v_cndmask_b32_e64 %[o6], %[o6], 2.0, s[86:87]\n\t"
        "v_cndmask_b32_e64 %[o7], %[o7], 2.0, s[88:89]\n\t"
        "v_cndmask_b32_e64 %[o8], %[o8], 2.0, s[90:91]\n\t"
        "v_min3_u32 v78, %[o0], %[o1], %[o2]\n\t" "v_min3_u32 v79, %[o3], %[o4], %[o5]\n\t" "v_min3_u32 v80, %[o6], %[o7], %[o8]\n\t" "v_min3_u32 v78, v78, v79, v80\n\t"
        "v_cmp_eq_u32 vcc, 2.0, v78\n\t"
        "v_add_f32 v79, v120, v124\n\t" "v_add_f32 v80, v122, v126\n\t"
        "v_cndmask_b32_e64 %[a0], %[a0], v88, s[74:75]\n\t" "v_cndmask_b32_e64 %[b0], %[b0], v90, s[74:75]\n\t"
        "v_cndmask_b32_e64 %[a1], %[a1], v92, s[76:77]\n\t" "v_cndmask_b32_e64 %[b1], %[b1], v94, s[76:77]\n\t"
        "v_cndmask_b32_e64 %[a2], %[a2], v96, s[78:79]\n\t" "v_cndmask_b32_e64 %[b2], %[b2], v98, s[78:79]\n\t"
        "v_cndmask_b32_e64 %[a3], %[a3], v100, s[80:81]\n\t" "v_cndmask_b32_e64 %[b3], %[b3], v102, s[80:81]\n\t"
        "v_cndmask_b32_e64 %[a4], %[a4], v104, s[82:83]\n\t" "v_cndmask_b32_e64 %[b4], %[b4], v106, s[82:83]\n\t"
        "v_cndmask_b32_e64 %[a5], %[a5], v108, s[84:85]\n\t" "v_cndmask_b32_e64 %[b5], %[b5], v110, s[84:85]\n\t"
        "v_cndmask_b32_e64 %[a6], %[a6], v112, s[86:87]\n\t" "v_cndmask_b32_e64 %[b6], %[b6], v114, s[86:87]\n\t"
        "v_cndmask_b32_e64 %[a7], %[a7], v116, s[88:89]\n\t" "v_cndmask_b32_e64 %[b7], %[b7], v118, s[88:89]\n\t"
        "v_cndmask_b32_e64 %[f0], %[f0], v79, s[90:91]\n\t" "v_cndmask_b32_e64 %[f1], %[f1], v80, s[90:91]\n\t"
        "s_cmp_eq_u64 vcc, exec\n\t" "s_cbranch_scc1 2f\n\t"
        "buffer_load_dwordx4 v[88:91], %[o0], %[d], 0 offen sc1\n\t"
        "buffer_load_dwordx4 v[92:95], %[o1], %[d], 0 offen sc1\n\t"
        "buffer_load_dwordx4 v[96:99], %[o2], %[d], 0 offen sc1\n\t"
        "buffer_load_dwordx4 v[100:103], %[o3], %[d], 0 offen sc1\n\t"
        "buffer_load_dwordx4 v[104:107], %[o4], %[d], 0 offen sc1\n\t"
        "buffer_load_dwordx4 v[108:111], %[o5], %[d], 0 offen sc1\n\t"
        "buffer_load_dwordx4 v[112:115], %[o6], %[d], 0 offen sc1\n\t"
        "buffer_load_dwordx4 v[116:119], %[o7], %[d], 0 offen sc1\n\t"
        "buffer_load_dwordx4 v[120:123], %[o8], %[d], 0 offen sc1\n\t"
        "buffer_load_dwordx4 v[124:127], %[o8], %[d], s73 offen sc1\n\t"
        "s_waitcnt vmcnt(10)\n\t"
        "v_bitop3_b32 v78, v129, v131, %[t] bitop3:0x7e\n\t"
        "v_bitop3_b32 v79, v133, v135, %[t] bitop3:0x7e\n\t"
        "v_bitop3_b32 v80, v137, v139, %[t] bitop3:0x7e\n\t"
        "v_bitop3_b32 v81, v141, v143, %[t] bitop3:0x7e\n\t"
        "v_bitop3_b32 v82, v145, v147, %[t] bitop3:0x7e\n\t"
        "v_bitop3_b32 v83, v149, v151, %[t] bitop3:0x7e\n\t"
        "v_bitop3_b32 v84, v153, v155, %[t] bitop3:0x7e\n\t"
        "v_bitop3_b32 v85, v157, v159, %[t] bitop3:0x7e\n\t"
        "v_bitop3_b32 v86, v161, v163, %[t] bitop3:0x7e\n\t"
        "v_bitop3_b32 v87, v165, v167, %[t] bitop3:0x7e\n\t"
        "v_or_b32 v86, v86, v87\n\t"
        "v_cmp_eq_u32 s[74:75], 0, v78\n\t"
        "v_cmp_eq_u32 s[76:77], 0, v79\n\t"
        "v_cmp_eq_u32 s[78:79], 0, v80\n\t"
        "v_cmp_eq_u32 s[80:81], 0, v81\n\t"
        "v_cmp_eq_u32 s[82:83], 0, v82\n\t"
        "v_cmp_eq_u32 s[84:85], 0, v83\n\t"
        "v_cmp_eq_u32 s[86:87], 0, v84\n\t"
        "v_cmp_eq_u32 s[88:89], 0, v85\n\t"
        "v_cmp_eq_u32 s[90:91], 0, v86\n\t"
        "v_cndmask_b32_e64 %[o0], %[o0], 2.0, s[74:75]\n\t"
        "v_cndmask_b32_e64 %[o1], %[o1], 2.0, s[76:77]\n\t"
        "v_cndmask_b32_e64 %[o2], %[o2], 2.0, s[78:79]\n\t"
        "v_cndmask_b32_e64 %[o3], %[o3], 2.0, s[80:81]\n\t"
        "v_cndmask_b32_e64 %[o4], %[o4], 2.0, s[82:83]\n\t"
        "v_cndmask_b32_e64 %[o5], %[o5], 2.0, s[84:85]\n\t"
        "v_cndmask_b32_e64 %[o6], %[o6], 2.0, s[86:87]\n\t"
        "v_cndmask_b32_e64 %[o7], %[o7], 2.0, s[88:89]\n\t"
        "v_cndmask_b32_e64 %[o8], %[o8], 2.0, s[90:91]\n\t"
        "v_min3_u32 v78, %[o0], %[o1], %[o2]\n\t" "v_min3_u32 v79, %[o3], %[o4], %[o5]\n\t" "v_min3_u32 v80, %[o6], %[o7], %[o8]\n\t" "v_min3_u32 v78, v78, v79, v80\n\t"
        "v_cmp_eq_u32 vcc, 2.0, v78\n\t"
        "v_add_f32 v79, v160, v164\n\t" "v_add_f32 v80, v162, v166\n\t"
        "v_cndmask_b32_e64 %[a0], %[a0], v128, s[74:75]\n\t" "v_cndmask_b32_e64 %[b0], %[b0], v130, s[74:75]\n\t"
        "v_cndmask_b32_e64 %[a1], %[a1], v132, s[76:77]\n\t" "v_cndmask_b32_e64 %[b1], %[b1], v134, s[76:77]\n\t"
        "v_cndmask_b32_e64 %[a2], %[a2], v136, s[78:79]\n\t" "v_cndmask_b32_e64 %[b2], %[b2], v138, s[78:79]\n\t"
        "v_cndmask_b32_e64 %[a3], %[a3], v140, s[80:81]\n\t" "v_cndmask_b32_e64 %[b3], %[b3], v142, s[80:81]\n\t"
        "v_cndmask_b32_e64 %[a4], %[a4], v144, s[82:83]\n\t" "v_cndmask_b32_e64 %[b4], %[b4], v146, s[82:83]\n\t"
        "v_cndmask_b32_e64 %[a5], %[a5], v148, s[84:85]\n\t" "v_cndmask_b32_e64 %[b5], %[b5], v150, s[84:85]\n\t"
        "v_cndmask_b32_e64 %[a6], %[a6], v152, s[86:87]\n\t" "v_cndmask_b32_e64 %[b6], %[b6], v154, s[86:87]\n\t"
        "v_cndmask_b32_e64 %[a7], %[a7], v156, s[88:89]\n\t" "v_cndmask_b32_e64 %[b7], %[b7], v158, s[88:89]\n\t"
        "v_cndmask_b32_e64 %[f0], %[f0], v79, s[90:91]\n\t" "v_cndmask_b32_e64 %[f1], %[f1], v80, s[90:91]\n\t"
        "s_cmp_eq_u64 vcc, exec\n\t" "s_cbranch_scc1 2f\n\t"
        "s_sub_u32 s72, s72, 1\n\t" "s_cbranch_scc0 1b\n\t"
        "2:\n\t" "s_nop 1 ; POLL_END"
        : [o0] "+v"(o[0]), [o1] "+v"(o[1]), [o2] "+v"(o[2]), [o3] "+v"(o[3]), [o4] "+v"(o[4]), [o5] "+v"(o[5]), [o6] "+v"(o[6]), [o7] "+v"(o[7]), [o8] "+v"(o[8]),
          [a0] "+v"(pa[0]), [a1] "+v"(pa[1]), [a2] "+v"(pa[2]), [a3] "+v"(pa[3]), [a4] "+v"(pa[4]), [a5] "+v"(pa[5]), [a6] "+v"(pa[6]), [a7] "+v"(pa[7]),
          [b0] "+v"(pb[0]), [b1] "+v"(pb[1]), [b2] "+v"(pb[2]), [b3] "+v"(pb[3]), [b4] "+v"(pb[4]), [b5] "+v"(pb[5]), [b6] "+v"(pb[6]), [b7] "+v"(pb[7]), [f0] "+v"(f0), [f1] "+v"(f1)
        : [d] "s"(dws), [t] "v"(tag)
        : "memory", "vcc", "scc", "s72", "s73", "s74", "s75", "s76", "s77", "s78", "s79", "s80", "s81", "s82", "s83", "s84", "s85", "s86", "s87", "s88", "s89", "s90", "s91", "v78", "v79", "v80", "v81", "v82", "v83", "v84", "v85", "v86", "v87", "v88", "v89", "v90", "v91", "v92", "v93", "v94", "v95", "v96", "v97", "v98", "v99", "v100", "v101", "v102", "v103", "v104", "v105", "v106", "v107", "v108", "v109", "v110", "v111", "v112", "v113", "v114", "v115", "v116", "v117", "v118", "v119", "v120", "v121", "v122", "v123", "v124", "v125", "v126", "v127", "v128", "v129", "v130", "v131", "v132", "v133", "v134", "v135", "v136", "v137", "v138", "v139", "v140", "v141", "v142", "v143", "v144", "v145", "v146", "v147", "v148", "v149", "v150", "v151", "v152", "v153", "v154", "v155", "v156", "v157", "v158", "v159", "v160", "v161", "v162", "v163", "v164", "v165", "v166", "v167");
    const float s0 = ((pa[0] + pa[1]) + (pa[2] + pa[3])) + ((pa[4] + pa[5]) + (pa[6] + pa[7]));
    const float s1 = ((pb[0] + pb[1]) + (pb[2] + pb[3])) + ((pb[4] + pb[5]) + (pb[6] + pb[7]));
    STAMP(6);
    const float f = rbf(pair_sum(f0, f1)), gate = rbf(f * rcp(1.f + ex2(-LOG2E * f)));
    const float res = xres + rbf(rbf(pair_sum(s0, s1)) * gate);
    __builtin_amdgcn_raw_buffer_store_b16((u16)cvtpk(res, res), rout, oo, 0, 0);
    __builtin_amdgcn_raw_buffer_store_b32(tag, rws, oe, 0, SC1);
    STAMP(7);
    asm volatile("s_waitcnt vmcnt(0) ; POLL_DRAIN" ::: "memory");
    EXIT_WAVE
  }

  LANE_IDS
  const int w = wave;
  __builtin_amdgcn_s_setprio(2);
  const int rowl = 32 * (n >> 3) + 8 * g, chk = (n & 7) * 16;
  const int afo = ((n >> 2) & 1) * 64 + g * 16;
  f4 acc[8];
#pragma unroll
  for (int j = 0; j < 8; ++j) acc[j] = f4{0.f, 0.f, 0.f, 0.f};
  if (__builtin_expect(c < N_S1, 1)) {
    const u16* base = c < 64 ? wq + c * 64 : c < 136 ? wg + (c - 64) * 64 : wu + (c - 136) * 64;
    const int stride = c < 64 ? NQ * 2 : FF * 2;
    const __amdgpu_buffer_rsrc_t rw = mkrsrc(base, (DM - 1) * stride + 128);
    const int voff = rowl * stride + chk;
    const unsigned tbase = tstart + (unsigned)(((2 * w - AHEAD) * TAU) >> 8);
    v4u R[24];
#pragma unroll
    for (int i = 0; i < 24; ++i) {
      if ((i & 1) == 0 && 3 * i > AHEAD) PACE(3 * i);
      R[i] = ld_nt(rw, voff, (192 * w + 64 * (i >> 3) + (i & 7)) * stride);
    }
    __builtin_amdgcn_sched_barrier(0);
    STAMP(1);
    lds_wait<1>(FL + F_XN, 1);
#pragma unroll
    for (int gi = 0; gi < 3; ++gi)
      gemv_group(R + 8 * gi, *(const LAS v4u*)(L + L_XN + (n & 3) * XN_STR + (3 * w + gi) * 128 + afo), acc);
    *(LAS f4*)(L + L_S1R + w * 1024 + lane * 16) = gemv_finish(acc, lane);
    const unsigned tag = FL[F_TAG];
    if (lds_arrive(F_S1) == 2) {
      const f4 o = (*(const LAS f4*)(L + L_S1R + lane * 16) + *(const LAS f4*)(L + L_S1R + 1024 + lane * 16)) + *(const LAS f4*)(L + L_S1R + 2048 + lane * 16);
      st_sc1(v4u{cvtpk(o[0], o[1]), tag, cvtpk(o[2], o[3]), tag}, rws,
             WS_S1 + (2 * (n >> 3) + (g & 1)) * S1_ROW + (64 * c + 8 * (n & 7) + 4 * (g >> 1)) * 4);
    }
    STAMP(2);
    if (c >= N_WO) EXIT_WAVE
    lds_wait<1>(FL + F_D, WOCNT);
    const __amdgpu_buffer_rsrc_t ro = mkrsrc(wo + (size_t)ksl * 64 * DM + (3 * cs + w) * 64, 63 * DM * 2 + 128);
    v4u W[8], T[8];
#pragma unroll
    for (int i = 0; i < 8; ++i) W[i] = ld_nt(ro, rowl * (DM * 2) + chk + i * (DM * 2));
#pragma unroll
    for (int q = 0; q < 4; ++q)
#pragma unroll
      for (int p = 0; p < 4; ++p) {
        T[2 * q][p] = __builtin_amdgcn_perm(W[2 * p + 1][q], W[2 * p][q], 0x05040100u);
        T[2 * q + 1][p] = __builtin_amdgcn_perm(W[2 * p + 1][q], W[2 * p][q], 0x07060302u);
      }
    int rec = WS_H2 + (cs * 64 + ksl) * 6144 + w * 2048 + lane * 16;
    asm volatile("" : "+v"(T[0]), "+v"(T[1]), "+v"(T[2]), "+v"(T[3]), "+v"(T[4]), "+v"(T[5]), "+v"(T[6]), "+v"(T[7]), "+v"(rec));
    STAMP(3);
    v4u alo, ahi;
    {
      const int af = L_AF + lane * 16;
      unsigned f_, s_;
      asm volatile("s_mov_b32 s72, %7\n1:\n\tds_read_b32 %2, %4\n\tds_read_b128 %0, %5\n\tds_read_b128 %1, %6\n\ts_waitcnt lgkmcnt(2)\n\t"
                   "v_readfirstlane_b32 %3, %2\n\ts_cmp_lg_u32 %3, 0\n\ts_cbranch_scc1 2f\n\ts_sub_u32 s72, s72, 1\n\ts_cbranch_scc0 1b\n2:\n\ts_waitcnt lgkmcnt(0)"
                   : "=&v"(alo), "=&v"(ahi), "=&v"(f_), "=&s"(s_)
                   : "v"(L_FLAG + 4 * F_A), "v"(n < 8 ? af : L_ZERO), "v"(n < 8 ? L_ZERO : af), "i"(LDS_SPIN) : "memory", "scc", "s72");
    }
#pragma unroll
    for (int j = 0; j < 4; ++j) acc[j] = mfma(alo, T[j], f4{0.f, 0.f, 0.f, 0.f});
#pragma unroll
    for (int j = 0; j < 4; ++j) acc[j] = mfma(ahi, T[j + 4], acc[j]);
    const f4 G = gemv_fold(acc, lane);
    st_sc1(v4u{__float_as_uint(G[0]), tag, __float_as_uint(G[1]), tag}, rws, rec);
    st_sc1(v4u{__float_as_uint(G[2]), tag, __float_as_uint(G[3]), tag}, rws, rec + 1024);
    lds_post(F_H2);
    STAMP(5);
  } else {
    const int ds = (c - N_S1) % 3, rs = (c - N_S1) / 3;
    const __amdgpu_buffer_rsrc_t rd = mkrsrc(wd + (size_t)rs * 288 * DM + (3 * ds + w) * 64, 287 * DM * 2 + 128);
    const int voff = rowl * (DM * 2) + chk, voff4 = n >= 8 ? OOB : voff;
    lds_wait<1>(FL + F_ISS, 8);
    v4u R[24];
#pragma unroll
    for (int i = 0; i < 24; ++i) R[i] = ld_nt(rd, voff, (64 * (i >> 3) + (i & 7)) * (DM * 2));
    __builtin_amdgcn_sched_barrier(0);
    STAMP(1);
    lds_wait<1>(FL + F_H, 1);
    STAMP(2);
    const unsigned tag = FL[F_TAG];
    const int ao = L_H + (n & 3) * H_STR + afo;
    gemv_group(R, *(const LAS v4u*)(L + ao), acc);
    __builtin_amdgcn_sched_barrier(0);
#pragma unroll
    for (int i = 0; i < 8; ++i) R[i] = ld_nt(rd, voff, (192 + i) * (DM * 2));
    __builtin_amdgcn_sched_barrier(0);
    gemv_group(R + 8, *(const LAS v4u*)(L + ao + 128), acc);
    __builtin_amdgcn_sched_barrier(0);
#pragma unroll
    for (int i = 0; i < 8; ++i) R[8 + i] = ld_nt(rd, voff4, (256 + i) * (DM * 2));
    __builtin_amdgcn_sched_barrier(0);
    gemv_group(R + 16, *(const LAS v4u*)(L + ao + 256), acc);
    gemv_group(R, *(const LAS v4u*)(L + ao + 384), acc);
    gemv_group(R + 8, *(const LAS v4u*)(L + ao + 512), acc);
    const f4 G = gemv_finish(acc, lane);
    const int rec = WS_F2 + (ds * 16 + rs) * 6144 + w * 2048 + lane * 16;
    st_sc1(v4u{__float_as_uint(G[0]), tag, __float_as_uint(G[1]), tag}, rws, rec);
    st_sc1(v4u{__float_as_uint(G[2]), tag, __float_as_uint(G[3]), tag}, rws, rec + 1024);
    STAMP(5);
  }
  STAMP_FLUSH();
}

extern "C" void kernel_launch(void* const* d_in, const int* in_sizes, int n_in, void* d_out, int out_size, void* d_ws,
                              size_t ws_size, hipStream_t stream) {
  fused_decode<<<NCU, NTHR, 0, stream>>>((const u16*)d_in[0], (const u16*)d_in[1], (const u16*)d_in[2], (const u16*)d_in[3],
                                         (const u16*)d_in[4], (const u16*)d_in[6], (const u16*)d_in[7], (const u16*)d_in[8],
                                         (u16*)d_out, (unsigned char*)d_ws);
}
```

```cpp
#include <hip/hip_runtime.h>
#include <stdint.h>

typedef unsigned short u16;
typedef unsigned v4u __attribute__((ext_vector_type(4)));
typedef unsigned v2u __attribute__((ext_vector_type(2)));
typedef float f4 __attribute__((ext_vector_type(4)));
typedef short s8v __attribute__((ext_vector_type(8)));
typedef __bf16 bf2v __attribute__((ext_vector_type(2)));
#define LAS __attribute__((address_space(3)))
#define GAS __attribute__((address_space(1)))
#define STR2(x) #x
#define STR(x) STR2(x)

constexpr int DM = 576, SK = 8192, WIN = 2048, FF = 4608, NQ = 4096;
constexpr int NCU = 256, NTHR = 768;
constexpr int N_S1 = 208, N_WO = 192, N_FIN = 144;
constexpr int OOB = 0x40000000;
constexpr int NT = 2, SC1 = 16;
constexpr float LOG2E = 1.4426950408889634f, QK_C = 0.08838834764831845f * LOG2E;

#define TAU 1000
#define AHEAD 24
#define VSPLIT 10
#define WOCNT 4
#define STAG 6
#define CODEPF 20480

constexpr int S1_ROW = (NQ + 2 * FF) * 4;
constexpr int WS_EPOCH = 0, WS_S1 = 4096, WS_H1 = WS_S1 + 4 * S1_ROW, WS_H2 = WS_H1 + NCU * 1024,
              WS_F2 = WS_H2 + 3 * 64 * 6144, WS_END = WS_F2 + 3 * 16 * 6144;
constexpr int XN_STR = 1168, H_STR = 656;
constexpr int L_XN = 0, L_H = L_XN + 4 * XN_STR, L_Q = L_H + 4 * H_STR, L_S1R = L_Q + 512, L_AM = L_S1R + 3 * 1024,
              L_AL = L_AM + 64, L_AD = L_AL + 64, L_AF = L_AD + 8 * 1024, L_XS = L_AF + 1024, L_DUM = L_XS + 5376,
              L_ZERO = L_DUM + 1024, L_FLAG = L_ZERO + 256, L_END = L_FLAG + 64;
enum { F_XN = 0, F_TAG, F_Q, F_H, F_S1, F_ISS, F_D, F_PUB, F_L, F_A, F_H2 };
static_assert(L_H % 16 == 0 && L_Q % 16 == 0 && L_S1R % 16 == 0 && L_AM % 16 == 0 && L_AD % 16 == 0 && L_XS % 16 == 0 &&
              L_DUM % 16 == 0 && L_ZERO % 16 == 0, "align");
constexpr unsigned LDS_SPIN = 1u << 22, GLB_SPIN = 1u << 16;

constexpr int WS_STAMP = 4 << 20;
#define STAMP(i) do {} while (0)
#define STAMP_FLUSH() do {} while (0)

#define PACE(kb) asm volatile("1:\n\ts_memrealtime s[92:93]\n\ts_waitcnt lgkmcnt(0)\n\ts_sub_u32 s92, s92, %0\n\ts_cmp_lt_i32 s92, %1\n\t" \
    "s_cbranch_scc0 2f\n\ts_sleep 1\n\ts_branch 1b\n2:" :: "s"(tbase), "i"((int)(((kb) * TAU) >> 8)) : "scc", "memory", "s92", "s93")
#define PLOAD(d, voff, soff) asm volatile("buffer_load_dwordx4 %0, %1, %2, %3 offen sc1" : "=v"(d) : "v"(voff), "s"(dws), "s"(soff) : "memory")
#define EXIT_WAVE { STAMP_FLUSH(); __builtin_amdgcn_endpgm(); __builtin_unreachable(); }

__device__ __forceinline__ __amdgpu_buffer_rsrc_t mkrsrc(const void* p, unsigned bytes) {
  return __builtin_amdgcn_make_buffer_rsrc((void*)p, 0, bytes, 0x00020000);
}
__device__ __forceinline__ v4u ld_nt(__amdgpu_buffer_rsrc_t r, int voff, int soff = 0) {
  return __builtin_amdgcn_raw_buffer_load_b128(r, voff, soff, NT);
}
__device__ __forceinline__ void st_sc1(v4u v, __amdgpu_buffer_rsrc_t r, int voff) {
  __builtin_amdgcn_raw_buffer_store_b128(v, r, voff, 0, SC1);
}
__device__ __forceinline__ f4 mfma(v4u a, v4u b, f4 c) {
  return __builtin_amdgcn_mfma_f32_16x16x32_bf16(__builtin_bit_cast(s8v, a), __builtin_bit_cast(s8v, b), c, 0, 0, 0);
}
__device__ __forceinline__ unsigned cvtpk(float lo, float hi) {
  bf2v r = {(__bf16)lo, (__bf16)hi};
  return __builtin_bit_cast(unsigned, r);
}
__device__ __forceinline__ float rbf(float f) { return (float)(__bf16)f; }
__device__ __forceinline__ float rcp(float f) { return __builtin_amdgcn_rcpf(f); }
__device__ __forceinline__ float ex2(float f) { return __builtin_amdgcn_exp2f(f); }
__device__ __forceinline__ float blo(unsigned u) { return __uint_as_float(u << 16); }
__device__ __forceinline__ float bhi(unsigned u) { return __uint_as_float(u & 0xffff0000u); }
__device__ __forceinline__ unsigned mktag(unsigned e) { return ((e + 1u) & 0xffffu) ? e + 1u : e + 2u; }
template <int CTRL> __device__ __forceinline__ float dpp(float x) {
  return __builtin_bit_cast(float, __builtin_amdgcn_mov_dpp(__builtin_bit_cast(int, x), CTRL, 0xf, 0xf, true));
}
constexpr int ROR1 = 0x121, ROR2 = 0x122, ROR4 = 0x124, ROR8 = 0x128, XOR1 = 0xB1;
__device__ __forceinline__ float row_sum(float x) { x += dpp<ROR8>(x); x += dpp<ROR4>(x); x += dpp<ROR2>(x); x += dpp<ROR1>(x); return x; }
__device__ __forceinline__ float row_max(float x) {
  x = fmaxf(x, dpp<ROR8>(x)); x = fmaxf(x, dpp<ROR4>(x)); x = fmaxf(x, dpp<ROR2>(x)); x = fmaxf(x, dpp<ROR1>(x)); return x; }
__device__ __forceinline__ void swap16(float& a, float& b) {
  auto s = __builtin_amdgcn_permlane16_swap(__float_as_uint(a), __float_as_uint(b), false, false);
  a = __uint_as_float(s[0]); b = __uint_as_float(s[1]);
}
__device__ __forceinline__ void swap32(float& a, float& b) {
  auto s = __builtin_amdgcn_permlane32_swap(__float_as_uint(a), __float_as_uint(b), false, false);
  a = __uint_as_float(s[0]); b = __uint_as_float(s[1]);
}
__device__ __forceinline__ float xrow_sum(float x) { float a = x, b = x; swap16(a, b); x = a + b; a = x; b = x; swap32(a, b); return a + b; }
__device__ __forceinline__ float xrow_max(float x) { float a = x, b = x; swap16(a, b); x = fmaxf(a, b); a = x; b = x; swap32(a, b); return fmaxf(a, b); }
__device__ __forceinline__ float pair_sum(float a, float b) {
  a += dpp<ROR8>(a); b += dpp<ROR8>(b);
  swap16(a, b);
  float t = a + b, u = t;
  swap32(t, u);
  return t + u;
}

template <int SLEEP> __device__ __forceinline__ void lds_wait(volatile LAS unsigned* f, unsigned want) {
#pragma nounroll
  for (unsigned s = 0; s < LDS_SPIN; ++s) {
    if ((unsigned)__builtin_amdgcn_readfirstlane(*f) >= want) break;
    if (SLEEP) __builtin_amdgcn_s_sleep(SLEEP);
  }
  asm volatile("" ::: "memory");
}
__device__ __forceinline__ void lds_post(int flag) {
  asm volatile("s_mov_b64 exec, 1\n\tds_add_u32 %0, %1\n\ts_mov_b64 exec, -1" :: "v"(L_FLAG + 4 * flag), "v"(1u) : "memory");
}
__device__ __forceinline__ unsigned lds_arrive(int flag) {
  unsigned o, r;
  asm volatile("s_mov_b64 exec, 1\n\tds_add_rtn_u32 %0, %2, %3\n\ts_mov_b64 exec, -1\n\ts_waitcnt lgkmcnt(0)\n\tv_readfirstlane_b32 %1, %0"
               : "=&v"(o), "=s"(r) : "v"(L_FLAG + 4 * flag), "v"(1u) : "memory");
  return r;
}

__device__ __forceinline__ void gemv_group(const v4u* r, v4u a, f4* acc) {
#pragma unroll
  for (int w = 0; w < 4; ++w) {
    v4u lo, hi;
#pragma unroll
    for (int p = 0; p < 4; ++p) {
      lo[p] = __builtin_amdgcn_perm(r[2 * p + 1][w], r[2 * p][w], 0x05040100u);
      hi[p] = __builtin_amdgcn_perm(r[2 * p + 1][w], r[2 * p][w], 0x07060302u);
    }
    acc[2 * w] = mfma(a, lo, acc[2 * w]);
    acc[2 * w + 1] = mfma(a, hi, acc[2 * w + 1]);
  }
}
__device__ __forceinline__ f4 gemv_fold(const f4* e, int lane) {
  f4 G;
#pragma unroll
  for (int j = 0; j < 4; ++j) {
    float e0 = e[j][0], e1 = e[j][1], e2 = e[j][2], e3 = e[j][3];
    swap16(e0, e1); swap16(e2, e3);
    const float t0 = e0 + dpp<ROR8>(e1), t1 = dpp<ROR8>(e2) + e3;
    G[j] = (lane & 8) ? t1 : t0;
  }
  return G;
}
__device__ __forceinline__ f4 gemv_finish(const f4* acc, int lane) {
  f4 e[4];
#pragma unroll
  for (int j = 0; j < 4; ++j) e[j] = lane < 32 ? acc[j] : acc[j + 4];
  return gemv_fold(e, lane);
}

__global__ void __launch_bounds__(NTHR, 3)
fused_decode(const u16* __restrict__ x, const u16* __restrict__ kc, const u16* __restrict__ vc, const u16* __restrict__ wq,
             const u16* __restrict__ wo, const u16* __restrict__ wg, const u16* __restrict__ wu, const u16* __restrict__ wd,
             u16* __restrict__ out, unsigned char* ws) {
  __shared__ __attribute__((aligned(16))) unsigned char lds_raw[L_END];
  LAS unsigned char* L = (LAS unsigned char*)lds_raw;
  volatile LAS unsigned* FL = (volatile LAS unsigned*)(L + L_FLAG);
  const unsigned tstart = (unsigned)__builtin_amdgcn_s_memrealtime();
  if (threadIdx.x < 80) *(volatile LAS unsigned*)(L + L_ZERO + threadIdx.x * 4) = 0u;
  asm volatile("s_waitcnt lgkmcnt(0)\n\ts_barrier" ::: "memory");
  asm volatile("" :: "s"(x), "s"(kc), "s"(vc), "s"(wq), "s"(wo), "s"(wg), "s"(wu), "s"(wd), "s"(out), "s"(ws));
  const int c = blockIdx.x, wave = __builtin_amdgcn_readfirstlane(threadIdx.x >> 6);
#define LANE_IDS int lane_ = threadIdx.x & 63; asm volatile("" : "+v"(lane_)); const int lane = lane_, g = lane >> 4, n = lane & 15;
  const int pair = (c & 7) * 8 + (c >> 5), sp = (c >> 3) & 3;
  const int ksl = (c & 7) * 8 + (c >> 3) / 3, cs = (c >> 3) % 3;
  const __amdgpu_buffer_rsrc_t rws = mkrsrc(ws, WS_END);
  if (wave == 3) {
    const v4u dx = {(unsigned)(uintptr_t)x, (unsigned)((uintptr_t)x >> 32), 4u * DM * 2u, 0x00020000u};
    const v4u de = {(unsigned)(uintptr_t)ws, (unsigned)((uintptr_t)ws >> 32), 4u, 0x00020000u};
    const int xo = (threadIdx.x & 63) * 16, z = 0;
    unsigned keep;
    asm volatile("s_mov_b32 %0, m0\n\ts_mov_b32 m0, %4\n\ts_nop 0\n\t"
                 "buffer_load_dwordx4 %1, %2, 0 offen lds\n\tbuffer_load_dwordx4 %1, %2, 0 offen offset:1024 lds\n\t"
                 "buffer_load_dwordx4 %1, %2, 0 offen offset:2048 lds\n\tbuffer_load_dwordx4 %1, %2, 0 offen offset:3072 lds\n\t"
                 "s_mov_b32 m0, %5\n\ts_movk_i32 s92, 0x1000\n\tbuffer_load_dwordx4 %1, %2, s92 offen lds\n\t"
                 "s_mov_b32 m0, %6\n\ts_nop 0\n\tbuffer_load_dword %7, %3, 0 offen sc1 lds\n\ts_mov_b32 m0, %0"
                 : "=&s"(keep) : "v"(xo), "s"(dx), "s"(de), "i"(L_XS), "i"(L_XS + 4096), "i"(L_XS + 5120), "v"(z) : "memory", "s92");
  }
  if (wave == 11) {
    const int po = (c >> 3) * 1024 + (threadIdx.x & 63) * 16;
    unsigned keep;
    asm volatile("s_mov_b32 %0, m0\n\ts_getpc_b64 s[92:93]\n\ts_and_b32 s92, s92, 0xffffff00\n\ts_mov_b32 s94, %2\n\ts_mov_b32 s95, 0x20000\n\t"
                 "s_mov_b32 m0, %3\n\ts_nop 0\n\tbuffer_load_dwordx4 %1, s[92:95], 0 offen lds\n\ts_mov_b32 m0, %0"
                 : "=&s"(keep) : "v"(po), "i"(CODEPF), "i"(L_DUM) : "memory", "scc", "s92", "s93", "s94", "s95");
  }

  if (__builtin_expect(wave >= 4, 0)) {
    LANE_IDS
    const int w = wave - 4;
    const size_t kvo = ((size_t)pair * SK + (SK - WIN) + sp * 512 + ((w + pair) & 7) * 64) * 128;
    const __amdgpu_buffer_rsrc_t rk = mkrsrc(kc + kvo, 16384), rv = mkrsrc(vc + kvo, 16384);
    const int kvo0 = (n >> 2) * 4096 + (n >> 1) * 256 + (n & 1) * 64 + g * 16, vvo0 = g * 4096 + n * 16;
    const unsigned tbase = tstart + (unsigned)((((c < N_S1 ? 72 : 0) + 2 * w - AHEAD) * TAU) >> 8);
    v4u K[16], V[16];
#pragma unroll
    for (int i = 0; i < 16; ++i) {
      if ((i & 1) == 0) PACE(8 * i);
      K[i] = ld_nt(rk, (kvo0 ^ ((i >> 2) * 4096)) + ((i >> 1) & 1) * 2048 + (i & 1) * 128);
    }
#define V_LOAD(i) { if (((i) & 1) == 0) PACE(128 + 8 * (i)); V[i] = ld_nt(rv, (vvo0 ^ (((i) >> 2) * 4096)) + (((i) & 1) * 8 + ((i) >> 1)) * 256); }
#pragma unroll
    for (int i = 0; i < VSPLIT; ++i) V_LOAD(i)
    __builtin_amdgcn_sched_barrier(0);
    STAMP(2);
    float m0, m1; unsigned pk;
    auto scores = [&]() __attribute__((always_inline)) {
      f4 E0 = f4{0.f, 0.f, 0.f, 0.f}, E1 = f4{0.f, 0.f, 0.f, 0.f};
      const int qa = L_Q + ((n >> 1) & 1) * 256 + (n & 1) * 64 + g * 16;
#pragma unroll
      for (int tp = 0; tp < 4; ++tp) {
        const int ad = (n >> 2) == tp ? qa : L_ZERO;
        const v4u q0 = *(const LAS v4u*)(L + ad), q1 = *(const LAS v4u*)(L + ad + 128);
        E0 = mfma(q0, K[4 * tp], E0); E1 = mfma(q0, K[4 * tp + 2], E1);
        E0 = mfma(q1, K[4 * tp + 1], E0); E1 = mfma(q1, K[4 * tp + 3], E1);
      }
      const float s0 = ((n & 1) ? E1[1] : E0[0]) + dpp<XOR1>((n & 1) ? E0[1] : E1[0]);
      const float s1 = ((n & 1) ? E1[3] : E0[2]) + dpp<XOR1>((n & 1) ? E0[3] : E1[2]);
      m0 = xrow_max(row_max(s0)); m1 = xrow_max(row_max(s1));
      const float p0 = ex2((s0 - m0) * QK_C), p1 = ex2((s1 - m1) * QK_C);
      const float l0 = xrow_sum(row_sum(p0)), l1 = xrow_sum(row_sum(p1));
      if (lane == 0) {
        *(LAS float*)(L + L_AM + w * 4) = m0; *(LAS float*)(L + L_AM + 32 + w * 4) = m1;
        *(LAS float*)(L + L_AL + w * 4) = l0; *(LAS float*)(L + L_AL + 32 + w * 4) = l1;
      }
      lds_post(F_L);
      pk = cvtpk(p0, p1);
    };
    const bool early = __builtin_amdgcn_readfirstlane(FL[F_Q]) != 0;
    asm volatile("" ::: "memory");
    if (early) scores();
    STAMP(3);
    __builtin_amdgcn_sched_barrier(0);
#pragma unroll
    for (int i = VSPLIT; i < 16; ++i) V_LOAD(i)
    __builtin_amdgcn_sched_barrier(0);
    lds_post(F_ISS);
    STAMP(1);
    if (!early) { lds_wait<1>(FL + F_Q, 1); scores(); }
    lds_wait<1>(FL + F_L, 8);
    float scale; unsigned t1, t3;
    {
      const int h = g >> 1;
      const f4 ma = *(const LAS f4*)(L + L_AM + h * 32), mb = *(const LAS f4*)(L + L_AM + h * 32 + 16);
      const f4 la = *(const LAS f4*)(L + L_AL + h * 32), lb = *(const LAS f4*)(L + L_AL + h * 32 + 16);
      const float Mh = fmaxf(fmaxf(fmaxf(ma[0], ma[1]), fmaxf(ma[2], ma[3])), fmaxf(fmaxf(mb[0], mb[1]), fmaxf(mb[2], mb[3])));
      float Lh = 0.f;
#pragma unroll
      for (int k = 0; k < 4; ++k) Lh = fmaf(la[k], ex2((ma[k] - Mh) * QK_C), Lh);
#pragma unroll
      for (int k = 0; k < 4; ++k) Lh = fmaf(lb[k], ex2((mb[k] - Mh) * QK_C), Lh);
      scale = ex2(((h ? m1 : m0) - Mh) * QK_C) * rcp(Lh);
      const unsigned th = FL[F_TAG] << 16, ml = __float_as_uint((g & 1) ? Lh : Mh);
      t1 = th | (ml & 0xffffu); t3 = th | (ml >> 16);
    }
    int oh = WS_H1 + (pair * 4 + sp) * 1024 + (g >> 1) * 512 + n * 32 + (g & 1) * 16, la_ = L_AD + lane * 16;
    const unsigned hs = (n & 8) ? 0x0302u : 0x0100u, one = (n & 1) ? ((hs << 16) | 0x0c0cu) : (0x0c0c0000u | hs);
    unsigned sel[4];
#pragma unroll
    for (int r = 0; r < 4; ++r) sel[r] = ((n & 7) >> 1) == r ? one : 0x0c0c0c0cu;
    asm volatile("" : "+v"(scale), "+v"(t1), "+v"(t3), "+v"(oh), "+v"(la_));
    __builtin_amdgcn_sched_barrier(0);
    f4 O = f4{0.f, 0.f, 0.f, 0.f};
#define PV_STEP(i) { const unsigned pb = (unsigned)__builtin_amdgcn_mov_dpp((int)pk, 0x150 + (i), 0xf, 0xf, true); \
      v4u a; a[0] = __builtin_amdgcn_perm(pb, pb, sel[0]); a[1] = __builtin_amdgcn_perm(pb, pb, sel[1]); \
      a[2] = __builtin_amdgcn_perm(pb, pb, sel[2]); a[3] = __builtin_amdgcn_perm(pb, pb, sel[3]); O = mfma(a, V[i], O); }
    PV_STEP(0) PV_STEP(1) PV_STEP(2) PV_STEP(3) PV_STEP(4) PV_STEP(5) PV_STEP(6) PV_STEP(7)
    PV_STEP(8) PV_STEP(9) PV_STEP(10) PV_STEP(11) PV_STEP(12) PV_STEP(13) PV_STEP(14) PV_STEP(15)
    *(LAS f4*)(L + la_ + w * 1024) = O * scale;
    STAMP(4);
    if (lds_arrive(F_D) == 7) {
      f4 t[8];
#pragma unroll
      for (int k = 0; k < 8; ++k) t[k] = *(const LAS f4*)(L + la_ + k * 1024);
      const f4 o = ((t[0] + t[1]) + (t[2] + t[3])) + ((t[4] + t[5]) + (t[6] + t[7]));
      st_sc1(v4u{cvtpk(o[0], o[1]), t1, cvtpk(o[2], o[3]), t3}, rws, oh);
      FL[F_PUB] = 1;
      STAMP(5);
    }
    EXIT_WAVE
  }

  if (__builtin_expect(wave == 3, 0)) {
    __builtin_amdgcn_s_setprio(3);
    LANE_IDS
    const v4u dws = {(unsigned)(uintptr_t)ws, (unsigned)((uintptr_t)ws >> 32), (unsigned)WS_END, 0x00020000u};
    const int zero = 0;
    asm volatile("s_waitcnt vmcnt(0)" ::: "memory");
    const unsigned tag = mktag(*(const volatile LAS unsigned*)(L + L_XS + 5120));
    if (c < N_S1) {
      float xf[40], s = 0.f;
#pragma unroll
      for (int i = 0; i < 5; ++i) {
        const v4u X = *(const volatile LAS v4u*)(L + ((i == 4 && n >= 8) ? L_ZERO : L_XS + g * (DM * 2) + (n + 16 * i) * 16));
#pragma unroll
        for (int k = 0; k < 4; ++k) { xf[i * 8 + 2 * k] = blo(X[k]); xf[i * 8 + 2 * k + 1] = bhi(X[k]); }
      }
#pragma unroll
      for (int i = 0; i < 40; ++i) s += xf[i];
      const float mean = row_sum(s) * (1.f / DM);
      float v = 0.f;
#pragma unroll
      for (int i = 0; i < 40; ++i) { const float d = (i >= 32 && n >= 8) ? 0.f : xf[i] - mean; v = fmaf(d, d, v); }
      const float var = row_sum(v) * (1.f / DM);
      const float mb = rbf(mean), rden = rcp(rbf(__builtin_amdgcn_sqrtf(rbf(rbf(var) + 1e-5f))));
#pragma unroll
      for (int i = 0; i < 5; ++i) {
        v4u o;
#pragma unroll
        for (int k = 0; k < 4; ++k) o[k] = cvtpk(rbf(xf[i * 8 + 2 * k] - mb) * rden, rbf(xf[i * 8 + 2 * k + 1] - mb) * rden);
        if (i < 4 || n < 8) *(LAS v4u*)(L + L_XN + g * XN_STR + (n + 16 * i) * 16) = o;
      }
    }
    FL[F_TAG] = tag;
    asm volatile("" ::: "memory");
    FL[F_XN] = 1;
    STAMP(1);
    {
      const int off = WS_S1 + (pair >> 4) * S1_ROW + (pair & 15) * 1024 + lane * 16;
      v4u r;
#pragma nounroll
      for (unsigned s = 0; s < GLB_SPIN; ++s) {
        PLOAD(r, off, zero);
        asm volatile("s_waitcnt vmcnt(0)" : "+v"(r) :: "memory");
        if (__all(r[1] == tag && r[3] == tag)) break;
      }
      *(LAS v2u*)(L + L_Q + lane * 8) = v2u{r[0], r[2]};
      asm volatile("" ::: "memory");
      FL[F_Q] = 1;
    }
    STAMP(2);
    if (c >= N_S1) {
      const int rs = (c - N_S1) / 3, up = FF * 4;
      v4u G[5], U[5];
      int off[5], loff[5];
#pragma unroll
      for (int i = 0; i < 5; ++i) {
        const int t = min(lane + 64 * i, 287), b = t / 72, qd = t - b * 72;
        off[i] = WS_S1 + b * S1_ROW + (NQ + 288 * rs + 4 * qd) * 4; loff[i] = b * H_STR + qd * 8;
      }
#pragma nounroll
      for (unsigned s = 0; s < GLB_SPIN; ++s) {
#pragma unroll
        for (int i = 0; i < 5; ++i) { PLOAD(G[i], off[i], zero); PLOAD(U[i], off[i], up); }
        asm volatile("s_waitcnt vmcnt(0)" : "+v"(G[0]), "+v"(G[1]), "+v"(G[2]), "+v"(G[3]), "+v"(G[4]),
                     "+v"(U[0]), "+v"(U[1]), "+v"(U[2]), "+v"(U[3]), "+v"(U[4]) :: "memory");
        bool ok = true;
#pragma unroll
        for (int i = 0; i < 5; ++i) ok &= (G[i][1] == tag) & (G[i][3] == tag) & (U[i][1] == tag) & (U[i][3] == tag);
        if (__all(ok)) break;
        __builtin_amdgcn_s_sleep(4);
      }
      if (lane < 32) *(LAS v2u*)(L + L_H + (lane >> 3) * H_STR + 576 + (lane & 7) * 8) = v2u{0u, 0u};
#pragma unroll
      for (int i = 0; i < 5; ++i) {
        float hv[4];
#pragma unroll
        for (int k = 0; k < 4; ++k) {
          const unsigned gw = G[i][(k >> 1) * 2], uw = U[i][(k >> 1) * 2];
          const float a = (k & 1) ? bhi(gw) : blo(gw), u = (k & 1) ? bhi(uw) : blo(uw);
          const float z2 = (-2.f * 0.7978845608028654f * LOG2E) * (a + 0.044715f * a * a * a);
          hv[k] = rbf(a * rcp(1.f + ex2(z2))) * u;
        }
        *(LAS v2u*)(L + L_H + loff[i]) = v2u{cvtpk(hv[0], hv[1]), cvtpk(hv[2], hv[3])};
      }
      asm volatile("" ::: "memory");
      FL[F_H] = 1;
      STAMP(3);
    }
    if (c >= N_WO) EXIT_WAVE
    const int fcs = c / 48, fr = c - fcs * 48, fw = fr >> 4, fpc = (fr >> 3) & 1, fgr = fr & 7;
    int oo = ((2 * (fgr & 1) + ((fgr >> 1) & 1)) * DM + 64 * (3 * fcs + fw) + 8 * (lane & 7) + 4 * (fgr >> 2) + 2 * fpc + (g & 1)) * 2;
    oo = (c < N_FIN && (lane & 40) == 0) ? oo : OOB;
    float xres = blo(__builtin_amdgcn_raw_buffer_load_b16(mkrsrc(x, 4 * DM * 2), oo, 0, 0));
    asm volatile("" : "+v"(xres), "+v"(oo));
    {
      const int hh = ksl >> 1, pc = (((n & 3) * 16 + (hh >> 1)) * 4 + 2 * (n >> 3));
      const int o1 = WS_H1 + pc * 1024 + (hh & 1) * 512 + (8 * (ksl & 1) + 4 * ((n >> 2) & 1) + g) * 32;
      unsigned p[12];
#pragma unroll
      for (int k = 0; k < 12; ++k) p[k] = 0u;
      lds_wait<1>(FL + F_PUB, 1);
      STAMP(3);
      asm volatile(
        "s_mov_b64 s[70:71], 0\n\t" "s_mov_b32 s72, %[n]\n\t"
        "v_cndmask_b32_e64 v119, %[o1], 2.0, s[70:71]\n\t"
        "buffer_load_dwordx4 v[120:123], v119, %[d], 0 offen offset:0 sc1\n\t"
        "buffer_load_dwordx4 v[124:127], v119, %[d], 0 offen offset:16 sc1\n\t"
        "buffer_load_dwordx4 v[128:131], v119, %[d], 0 offen offset:1024 sc1\n\t"
        "buffer_load_dwordx4 v[132:135], v119, %[d], 0 offen offset:1040 sc1\n\t"
        "s_sleep " STR(STAG) "\n\t"
        "1:\n\t"
        "v_cndmask_b32_e64 v119, %[o1], 2.0, s[70:71]\n\t"
        "buffer_load_dwordx4 v[136:139], v119, %[d], 0 offen offset:0 sc1\n\t"
        "buffer_load_dwordx4 v[140:143], v119, %[d], 0 offen offset:16 sc1\n\t"
        "buffer_load_dwordx4 v[144:147], v119, %[d], 0 offen offset:1024 sc1\n\t"
        "buffer_load_dwordx4 v[148:151], v119, %[d], 0 offen offset:1040 sc1\n\t"
        "s_waitcnt vmcnt(4)\n\t"
        "v_bitop3_b32 v112, v121, v123, %[t] bitop3:0x7e\n\t"
        "v_bitop3_b32 v113, v125, v127, %[t] bitop3:0x7e\n\t"
        "v_bitop3_b32 v114, v129, v131, %[t] bitop3:0x7e\n\t"
        "v_bitop3_b32 v115, v133, v135, %[t] bitop3:0x7e\n\t"
        "v_or3_b32 v112, v112, v113, v114\n\t" "v_or_b32 v112, v112, v115\n\t" "v_cmp_gt_u32 vcc, 0x10000, v112\n\t"
        "v_perm_b32 v113, v123, v121, %[sl]\n\t"
        "v_perm_b32 v114, v127, v125, %[sl]\n\t"
        "v_perm_b32 v115, v131, v129, %[sl]\n\t"
        "v_perm_b32 v116, v135, v133, %[sl]\n\t"
        "s_or_b64 s[70:71], s[70:71], vcc\n\t"
        "v_cndmask_b32 %[p0], %[p0], v120, vcc\n\t"
        "v_cndmask_b32 %[p1], %[p1], v122, vcc\n\t"
        "v_cndmask_b32 %[p2], %[p2], v124, vcc\n\t"
        "v_cndmask_b32 %[p3], %[p3], v126, vcc\n\t"
        "v_cndmask_b32 %[p4], %[p4], v128, vcc\n\t"
        "v_cndmask_b32 %[p5], %[p5], v130, vcc\n\t"
        "v_cndmask_b32 %[p6], %[p6], v132, vcc\n\t"
        "v_cndmask_b32 %[p7], %[p7], v134, vcc\n\t"
        "v_cndmask_b32 %[p8], %[p8], v113, vcc\n\t"
        "v_cndmask_b32 %[p9], %[p9], v114, vcc\n\t"
        "v_cndmask_b32 %[p10], %[p10], v115, vcc\n\t"
        "v_cndmask_b32 %[p11], %[p11], v116, vcc\n\t"
        "s_cmp_eq_u64 s[70:71], exec\n\t" "s_cbranch_scc1 2f\n\t"
        "v_cndmask_b32_e64 v119, %[o1], 2.0, s[70:71]\n\t"
        "buffer_load_dwordx4 v[120:123], v119, %[d], 0 offen offset:0 sc1\n\t"
        "buffer_load_dwordx4 v[124:127], v119, %[d], 0 offen offset:16 sc1\n\t"
        "buffer_load_dwordx4 v[128:131], v119, %[d], 0 offen offset:1024 sc1\n\t"
        "buffer_load_dwordx4 v[132:135], v119, %[d], 0 offen offset:1040 sc1\n\t"
        "s_waitcnt vmcnt(4)\n\t"
        "v_bitop3_b32 v112, v137, v139, %[t] bitop3:0x7e\n\t"
        "v_bitop3_b32 v113, v141, v143, %[t] bitop3:0x7e\n\t"
        "v_bitop3_b32 v114, v145, v147, %[t] bitop3:0x7e\n\t"
        "v_bitop3_b32 v115, v149, v151, %[t] bitop3:0x7e\n\t"
        "v_or3_b32 v112, v112, v113, v114\n\t" "v_or_b32 v112, v112, v115\n\t" "v_cmp_gt_u32 vcc, 0x10000, v112\n\t"
        "v_perm_b32 v113, v139, v137, %[sl]\n\t"
        "v_perm_b32 v114, v143, v141, %[sl]\n\t"
        "v_perm_b32 v115, v147, v145, %[sl]\n\t"
        "v_perm_b32 v116, v151, v149, %[sl]\n\t"
        "s_or_b64 s[70:71], s[70:71], vcc\n\t"
        "v_cndmask_b32 %[p0], %[p0], v136, vcc\n\t"
        "v_cndmask_b32 %[p1], %[p1], v138, vcc\n\t"
        "v_cndmask_b32 %[p2], %[p2], v140, vcc\n\t"
        "v_cndmask_b32 %[p3], %[p3], v142, vcc\n\t"
        "v_cndmask_b32 %[p4], %[p4], v144, vcc\n\t"
        "v_cndmask_b32 %[p5], %[p5], v146, vcc\n\t"
        "v_cndmask_b32 %[p6], %[p6], v148, vcc\n\t"
        "v_cndmask_b32 %[p7], %[p7], v150, vcc\n\t"
        "v_cndmask_b32 %[p8], %[p8], v113, vcc\n\t"
        "v_cndmask_b32 %[p9], %[p9], v114, vcc\n\t"
        "v_cndmask_b32 %[p10], %[p10], v115, vcc\n\t"
        "v_cndmask_b32 %[p11], %[p11], v116, vcc\n\t"
        "s_cmp_eq_u64 s[70:71], exec\n\t" "s_cbranch_scc1 2f\n\t"
        "s_bcnt0_i32_b64 s73, s[70:71]\n\t" "s_cmp_lt_u32 s73, 17\n\t" "s_cbranch_scc1 3f\n\t" "s_sleep 12\n\t" "3:\n\t"
        "s_sub_u32 s72, s72, 1\n\t" "s_cbranch_scc0 1b\n\t"
        "2:\n\t" "s_nop 1 ; POLL_END"
        : [p0] "+v"(p[0]), [p1] "+v"(p[1]), [p2] "+v"(p[2]), [p3] "+v"(p[3]), [p4] "+v"(p[4]), [p5] "+v"(p[5]), [p6] "+v"(p[6]), [p7] "+v"(p[7]), [p8] "+v"(p[8]), [p9] "+v"(p[9]), [p10] "+v"(p[10]), [p11] "+v"(p[11])
        : [o1] "v"(o1), [d] "s"(dws), [t] "v"(tag << 16), [sl] "s"(0x05040100u), [n] "i"(GLB_SPIN)
        : "memory", "vcc", "scc", "s70", "s71", "s72", "s73", "v112", "v113", "v114", "v115", "v116", "v117", "v118", "v119", "v120", "v121", "v122", "v123", "v124", "v125", "v126", "v127", "v128", "v129", "v130", "v131", "v132", "v133", "v134", "v135", "v136", "v137", "v138", "v139", "v140", "v141", "v142", "v143", "v144", "v145", "v146", "v147", "v148", "v149", "v150", "v151");
      const float ma = __uint_as_float(p[8]), mb = __uint_as_float(p[10]);
      float M = fmaxf(ma, mb); M = fmaxf(M, dpp<ROR8>(M));
      const float ea = __uint_as_float(p[9]) * ex2((ma - M) * QK_C), eb = __uint_as_float(p[11]) * ex2((mb - M) * QK_C);
      float Ls = ea + eb; Ls += dpp<ROR8>(Ls);
      const float inv = rcp(Ls);
      v4u a;
#pragma unroll
      for (int k = 0; k < 4; ++k) {
        float lo = fmaf(ea, blo(p[k]), eb * blo(p[4 + k])), hi = fmaf(ea, bhi(p[k]), eb * bhi(p[4 + k]));
        lo += dpp<ROR8>(lo); hi += dpp<ROR8>(hi);
        a[k] = cvtpk(lo * inv, hi * inv);
      }
      *(LAS v4u*)(L + L_AF + lane * 16) = a;
      asm volatile("" ::: "memory");
      FL[F_A] = 1;
      STAMP(4);
      asm volatile("s_waitcnt vmcnt(0) ; POLL_DRAIN" ::: "memory");
    }
    if (c >= N_FIN) EXIT_WAVE
    const int lo_ = fw * 2048 + fpc * 1024 + fgr * 128 + (lane & 7) * 16;
    float gate;
    {
      const int o2 = WS_F2 + (fcs * 16 + (lane >> 3)) * 6144 + lo_, so = 8 * 6144;
      v4u Fa, Fb;
#pragma nounroll
      for (unsigned s = 0; s < GLB_SPIN; ++s) {
        PLOAD(Fa, o2, zero); PLOAD(Fb, o2, so);
        asm volatile("s_waitcnt vmcnt(0)" : "+v"(Fa), "+v"(Fb) :: "memory");
        if (__all((Fa[1] == tag) & (Fa[3] == tag) & (Fb[1] == tag) & (Fb[3] == tag))) break;
      }
      const float f = rbf(pair_sum(__uint_as_float(Fa[0]) + __uint_as_float(Fb[0]), __uint_as_float(Fa[2]) + __uint_as_float(Fb[2])));
      gate = rbf(f * rcp(1.f + ex2(-LOG2E * f)));
    }
    int o[8]; float pa[8], pb[8];
#pragma unroll
    for (int i = 0; i < 8; ++i) { o[i] = WS_H2 + (fcs * 64 + 8 * i + (lane >> 3)) * 6144 + lo_; pa[i] = 0.f; pb[i] = 0.f; }
    const __amdgpu_buffer_rsrc_t rout = mkrsrc(out, 4 * DM * 2);
    int oe = (c | lane) ? OOB : WS_EPOCH;
    asm volatile("" : "+v"(gate), "+v"(oe));
    lds_wait<0>(FL + F_H2, 3);
    asm volatile(
        "s_mov_b32 s72, %[n]\n\t"
        "buffer_load_dwordx4 v[104:107], %[o0], %[d], 0 offen sc1\n\t"
        "buffer_load_dwordx4 v[108:111], %[o1], %[d], 0 offen sc1\n\t"
        "buffer_load_dwordx4 v[112:115], %[o2], %[d], 0 offen sc1\n\t"
        "buffer_load_dwordx4 v[116:119], %[o3], %[d], 0 offen sc1\n\t"
        "buffer_load_dwordx4 v[120:123], %[o4], %[d], 0 offen sc1\n\t"
        "buffer_load_dwordx4 v[124:127], %[o5], %[d], 0 offen sc1\n\t"
        "buffer_load_dwordx4 v[128:131], %[o6], %[d], 0 offen sc1\n\t"
        "buffer_load_dwordx4 v[132:135], %[o7], %[d], 0 offen sc1\n\t"
        "s_sleep " STR(STAG) "\n\t"
        "1:\n\t"
        "buffer_load_dwordx4 v[136:139], %[o0], %[d], 0 offen sc1\n\t"
        "buffer_load_dwordx4 v[140:143], %[o1], %[d], 0 offen sc1\n\t"
        "buffer_load_dwordx4 v[144:147], %[o2], %[d], 0 offen sc1\n\t"
        "buffer_load_dwordx4 v[148:151], %[o3], %[d], 0 offen sc1\n\t"
        "buffer_load_dwordx4 v[152:155], %[o4], %[d], 0 offen sc1\n\t"
        "buffer_load_dwordx4 v[156:159], %[o5], %[d], 0 offen sc1\n\t"
        "buffer_load_dwordx4 v[160:163], %[o6], %[d], 0 offen sc1\n\t"
        "buffer_load_dwordx4 v[164:167], %[o7], %[d], 0 offen sc1\n\t"
        "s_waitcnt vmcnt(8)\n\t"
        "v_bitop3_b32 v92, v105, v107, %[t] bitop3:0x7e\n\t"
        "v_bitop3_b32 v93, v109, v111, %[t] bitop3:0x7e\n\t"
        "v_bitop3_b32 v94, v113, v115, %[t] bitop3:0x7e\n\t"
        "v_bitop3_b32 v95, v117, v119, %[t] bitop3:0x7e\n\t"
        "v_bitop3_b32 v96, v121, v123, %[t] bitop3:0x7e\n\t"
        "v_bitop3_b32 v97, v125, v127, %[t] bitop3:0x7e\n\t"
        "v_bitop3_b32 v98, v129, v131, %[t] bitop3:0x7e\n\t"
        "v_bitop3_b32 v99, v133, v135, %[t] bitop3:0x7e\n\t"
        "v_cmp_eq_u32 s[76:77], 0, v92\n\t"
        "v_cmp_eq_u32 s[78:79], 0, v93\n\t"
        "v_cmp_eq_u32 s[80:81], 0, v94\n\t"
        "v_cmp_eq_u32 s[82:83], 0, v95\n\t"
        "v_cmp_eq_u32 s[84:85], 0, v96\n\t"
        "v_cmp_eq_u32 s[86:87], 0, v97\n\t"
        "v_cmp_eq_u32 s[88:89], 0, v98\n\t"
        "v_cmp_eq_u32 s[90:91], 0, v99\n\t"
        "v_cndmask_b32_e64 %[o0], %[o0], 2.0, s[76:77]\n\t"
        "v_cndmask_b32_e64 %[o1], %[o1], 2.0, s[78:79]\n\t"
        "v_cndmask_b32_e64 %[o2], %[o2], 2.0, s[80:81]\n\t"
        "v_cndmask_b32_e64 %[o3], %[o3], 2.0, s[82:83]\n\t"
        "v_cndmask_b32_e64 %[o4], %[o4], 2.0, s[84:85]\n\t"
        "v_cndmask_b32_e64 %[o5], %[o5], 2.0, s[86:87]\n\t"
        "v_cndmask_b32_e64 %[o6], %[o6], 2.0, s[88:89]\n\t"
        "v_cndmask_b32_e64 %[o7], %[o7], 2.0, s[90:91]\n\t"
        "v_min3_u32 v92, %[o0], %[o1], %[o2]\n\t" "v_min3_u32 v93, %[o3], %[o4], %[o5]\n\t" "v_min3_u32 v92, v92, %[o6], %[o7]\n\t" "v_min_u32 v92, v92, v93\n\t"
        "v_cmp_eq_u32 vcc, 2.0, v92\n\t"
        "v_cndmask_b32_e64 %[a0], %[a0], v104, s[76:77]\n\t" "v_cndmask_b32_e64 %[b0], %[b0], v106, s[76:77]\n\t"
        "v_cndmask_b32_e64 %[a1], %[a1], v108, s[78:79]\n\t" "v_cndmask_b32_e64 %[b1], %[b1], v110, s[78:79]\n\t"
        "v_cndmask_b32_e64 %[a2], %[a2], v112, s[80:81]\n\t" "v_cndmask_b32_e64 %[b2], %[b2], v114, s[80:81]\n\t"
        "v_cndmask_b32_e64 %[a3], %[a3], v116, s[82:83]\n\t" "v_cndmask_b32_e64 %[b3], %[b3], v118, s[82:83]\n\t"
        "v_cndmask_b32_e64 %[a4], %[a4], v120, s[84:85]\n\t" "v_cndmask_b32_e64 %[b4], %[b4], v122, s[84:85]\n\t"
        "v_cndmask_b32_e64 %[a5], %[a5], v124, s[86:87]\n\t" "v_cndmask_b32_e64 %[b5], %[b5], v126, s[86:87]\n\t"
        "v_cndmask_b32_e64 %[a6], %[a6], v128, s[88:89]\n\t" "v_cndmask_b32_e64 %[b6], %[b6], v130, s[88:89]\n\t"
        "v_cndmask_b32_e64 %[a7], %[a7], v132, s[90:91]\n\t" "v_cndmask_b32_e64 %[b7], %[b7], v134, s[90:91]\n\t"
        "s_cmp_eq_u64 vcc, exec\n\t" "s_cbranch_scc1 2f\n\t"
        "buffer_load_dwordx4 v[104:107], %[o0], %[d], 0 offen sc1\n\t"
        "buffer_load_dwordx4 v[108:111], %[o1], %[d], 0 offen sc1\n\t"
        "buffer_load_dwordx4 v[112:115], %[o2], %[d], 0 offen sc1\n\t"
        "buffer_load_dwordx4 v[116:119], %[o3], %[d], 0 offen sc1\n\t"
        "buffer_load_dwordx4 v[120:123], %[o4], %[d], 0 offen sc1\n\t"
        "buffer_load_dwordx4 v[124:127], %[o5], %[d], 0 offen sc1\n\t"
        "buffer_load_dwordx4 v[128:131], %[o6], %[d], 0 offen sc1\n\t"
        "buffer_load_dwordx4 v[132:135], %[o7], %[d], 0 offen sc1\n\t"
        "s_waitcnt vmcnt(8)\n\t"
        "v_bitop3_b32 v92, v137, v139, %[t] bitop3:0x7e\n\t"
        "v_bitop3_b32 v93, v141, v143, %[t] bitop3:0x7e\n\t"
        "v_bitop3_b32 v94, v145, v147, %[t] bitop3:0x7e\n\t"
        "v_bitop3_b32 v95, v149, v151, %[t] bitop3:0x7e\n\t"
        "v_bitop3_b32 v96, v153, v155, %[t] bitop3:0x7e\n\t"
        "v_bitop3_b32 v97, v157, v159, %[t] bitop3:0x7e\n\t"
        "v_bitop3_b32 v98, v161, v163, %[t] bitop3:0x7e\n\t"
        "v_bitop3_b32 v99, v165, v167, %[t] bitop3:0x7e\n\t"
        "v_cmp_eq_u32 s[76:77], 0, v92\n\t"
        "v_cmp_eq_u32 s[78:79], 0, v93\n\t"
        "v_cmp_eq_u32 s[80:81], 0, v94\n\t"
        "v_cmp_eq_u32 s[82:83], 0, v95\n\t"
        "v_cmp_eq_u32 s[84:85], 0, v96\n\t"
        "v_cmp_eq_u32 s[86:87], 0, v97\n\t"
        "v_cmp_eq_u32 s[88:89], 0, v98\n\t"
        "v_cmp_eq_u32 s[90:91], 0, v99\n\t"
        "v_cndmask_b32_e64 %[o0], %[o0], 2.0, s[76:77]\n\t"
        "v_cndmask_b32_e64 %[o1], %[o1], 2.0, s[78:79]\n\t"
        "v_cndmask_b32_e64 %[o2], %[o2], 2.0, s[80:81]\n\t"
        "v_cndmask_b32_e64 %[o3], %[o3], 2.0, s[82:83]\n\t"
        "v_cndmask_b32_e64 %[o4], %[o4], 2.0, s[84:85]\n\t"
        "v_cndmask_b32_e64 %[o5], %[o5], 2.0, s[86:87]\n\t"
        "v_cndmask_b32_e64 %[o6], %[o6], 2.0, s[88:89]\n\t"
        "v_cndmask_b32_e64 %[o7], %[o7], 2.0, s[90:91]\n\t"
        "v_min3_u32 v92, %[o0], %[o1], %[o2]\n\t" "v_min3_u32 v93, %[o3], %[o4], %[o5]\n\t" "v_min3_u32 v92, v92, %[o6], %[o7]\n\t" "v_min_u32 v92, v92, v93\n\t"
        "v_cmp_eq_u32 vcc, 2.0, v92\n\t"
        "v_cndmask_b32_e64 %[a0], %[a0], v136, s[76:77]\n\t" "v_cndmask_b32_e64 %[b0], %[b0], v138, s[76:77]\n\t"
        "v_cndmask_b32_e64 %[a1], %[a1], v140, s[78:79]\n\t" "v_cndmask_b32_e64 %[b1], %[b1], v142, s[78:79]\n\t"
        "v_cndmask_b32_e64 %[a2], %[a2], v144, s[80:81]\n\t" "v_cndmask_b32_e64 %[b2], %[b2], v146, s[80:81]\n\t"
        "v_cndmask_b32_e64 %[a3], %[a3], v148, s[82:83]\n\t" "v_cndmask_b32_e64 %[b3], %[b3], v150, s[82:83]\n\t"
        "v_cndmask_b32_e64 %[a4], %[a4], v152, s[84:85]\n\t" "v_cndmask_b32_e64 %[b4], %[b4], v154, s[84:85]\n\t"
        "v_cndmask_b32_e64 %[a5], %[a5], v156, s[86:87]\n\t" "v_cndmask_b32_e64 %[b5], %[b5], v158, s[86:87]\n\t"
        "v_cndmask_b32_e64 %[a6], %[a6], v160, s[88:89]\n\t" "v_cndmask_b32_e64 %[b6], %[b6], v162, s[88:89]\n\t"
        "v_cndmask_b32_e64 %[a7], %[a7], v164, s[90:91]\n\t" "v_cndmask_b32_e64 %[b7], %[b7], v166, s[90:91]\n\t"
        "s_cmp_eq_u64 vcc, exec\n\t" "s_cbranch_scc1 2f\n\t"
        "s_sub_u32 s72, s72, 1\n\t" "s_cbranch_scc0 1b\n\t"
        "2:\n\t" "s_nop 1 ; POLL_END"
        : [o0] "+v"(o[0]), [o1] "+v"(o[1]), [o2] "+v"(o[2]), [o3] "+v"(o[3]), [o4] "+v"(o[4]), [o5] "+v"(o[5]), [o6] "+v"(o[6]), [o7] "+v"(o[7]),
          [a0] "+v"(pa[0]), [a1] "+v"(pa[1]), [a2] "+v"(pa[2]), [a3] "+v"(pa[3]), [a4] "+v"(pa[4]), [a5] "+v"(pa[5]), [a6] "+v"(pa[6]), [a7] "+v"(pa[7]),
          [b0] "+v"(pb[0]), [b1] "+v"(pb[1]), [b2] "+v"(pb[2]), [b3] "+v"(pb[3]), [b4] "+v"(pb[4]), [b5] "+v"(pb[5]), [b6] "+v"(pb[6]), [b7] "+v"(pb[7])
        : [d] "s"(dws), [t] "v"(tag), [n] "i"(GLB_SPIN)
        : "memory", "vcc", "scc", "s72", "s76", "s77", "s78", "s79", "s80", "s81", "s82", "s83", "s84", "s85", "s86", "s87", "s88", "s89", "s90", "s91", "v92", "v93", "v94", "v95", "v96", "v97", "v98", "v99", "v100", "v101", "v102", "v103", "v104", "v105", "v106", "v107", "v108", "v109", "v110", "v111", "v112", "v113", "v114", "v115", "v116", "v117", "v118", "v119", "v120", "v121", "v122", "v123", "v124", "v125", "v126", "v127", "v128", "v129", "v130", "v131", "v132", "v133", "v134", "v135", "v136", "v137", "v138", "v139", "v140", "v141", "v142", "v143", "v144", "v145", "v146", "v147", "v148", "v149", "v150", "v151", "v152", "v153", "v154", "v155", "v156", "v157", "v158", "v159", "v160", "v161", "v162", "v163", "v164", "v165", "v166", "v167");
    const float s0 = ((pa[0] + pa[1]) + (pa[2] + pa[3])) + ((pa[4] + pa[5]) + (pa[6] + pa[7]));
    const float s1 = ((pb[0] + pb[1]) + (pb[2] + pb[3])) + ((pb[4] + pb[5]) + (pb[6] + pb[7]));
    STAMP(6);
    const float res = xres + rbf(rbf(pair_sum(s0, s1)) * gate);
    __builtin_amdgcn_raw_buffer_store_b16((u16)cvtpk(res, res), rout, oo, 0, 0);
    __builtin_amdgcn_raw_buffer_store_b32(tag, rws, oe, 0, SC1);
    STAMP(7);
    asm volatile("s_waitcnt vmcnt(0) ; POLL_DRAIN" ::: "memory");
    EXIT_WAVE
  }

  LANE_IDS
  const int w = wave;
  __builtin_amdgcn_s_setprio(2);
  const int rowl = 32 * (n >> 3) + 8 * g, chk = (n & 7) * 16;
  const int afo = ((n >> 2) & 1) * 64 + g * 16;
  f4 acc[8];
#pragma unroll
  for (int j = 0; j < 8; ++j) acc[j] = f4{0.f, 0.f, 0.f, 0.f};
  if (__builtin_expect(c < N_S1, 1)) {
    const u16* base = c < 64 ? wq + c * 64 : c < 136 ? wg + (c - 64) * 64 : wu + (c - 136) * 64;
    const int stride = c < 64 ? NQ * 2 : FF * 2;
    const __amdgpu_buffer_rsrc_t rw = mkrsrc(base, (DM - 1) * stride + 128);
    const int voff = rowl * stride + chk;
    const unsigned tbase = tstart + (unsigned)(((2 * w - AHEAD) * TAU) >> 8);
    v4u R[24];
#pragma unroll
    for (int i = 0; i < 24; ++i) {
      if ((i & 1) == 0 && 3 * i > AHEAD) PACE(3 * i);
      R[i] = ld_nt(rw, voff, (192 * w + 64 * (i >> 3) + (i & 7)) * stride);
    }
    __builtin_amdgcn_sched_barrier(0);
    STAMP(1);
    lds_wait<1>(FL + F_XN, 1);
#pragma unroll
    for (int gi = 0; gi < 3; ++gi)
      gemv_group(R + 8 * gi, *(const LAS v4u*)(L + L_XN + (n & 3) * XN_STR + (3 * w + gi) * 128 + afo), acc);
    *(LAS f4*)(L + L_S1R + w * 1024 + lane * 16) = gemv_finish(acc, lane);
    const unsigned tag = FL[F_TAG];
    if (lds_arrive(F_S1) == 2) {
      const f4 o = (*(const LAS f4*)(L + L_S1R + lane * 16) + *(const LAS f4*)(L + L_S1R + 1024 + lane * 16)) + *(const LAS f4*)(L + L_S1R + 2048 + lane * 16);
      st_sc1(v4u{cvtpk(o[0], o[1]), tag, cvtpk(o[2], o[3]), tag}, rws,
             WS_S1 + (2 * (n >> 3) + (g & 1)) * S1_ROW + (64 * c + 8 * (n & 7) + 4 * (g >> 1)) * 4);
    }
    STAMP(2);
    if (c >= N_WO) EXIT_WAVE
    lds_wait<1>(FL + F_D, WOCNT);
    const __amdgpu_buffer_rsrc_t ro = mkrsrc(wo + (size_t)ksl * 64 * DM + (3 * cs + w) * 64, 63 * DM * 2 + 128);
    v4u W[8], T[8];
#pragma unroll
    for (int i = 0; i < 8; ++i) W[i] = ld_nt(ro, rowl * (DM * 2) + chk + i * (DM * 2));
#pragma unroll
    for (int q = 0; q < 4; ++q)
#pragma unroll
      for (int p = 0; p < 4; ++p) {
        T[2 * q][p] = __builtin_amdgcn_perm(W[2 * p + 1][q], W[2 * p][q], 0x05040100u);
        T[2 * q + 1][p] = __builtin_amdgcn_perm(W[2 * p + 1][q], W[2 * p][q], 0x07060302u);
      }
    int rec = WS_H2 + (cs * 64 + ksl) * 6144 + w * 2048 + lane * 16;
    asm volatile("" : "+v"(T[0]), "+v"(T[1]), "+v"(T[2]), "+v"(T[3]), "+v"(T[4]), "+v"(T[5]), "+v"(T[6]), "+v"(T[7]), "+v"(rec));
    STAMP(3);
    v4u alo, ahi;
    {
      const int af = L_AF + lane * 16;
      unsigned f_, s_;
      asm volatile("s_mov_b32 s72, %7\n1:\n\tds_read_b32 %2, %4\n\tds_read_b128 %0, %5\n\tds_read_b128 %1, %6\n\ts_waitcnt lgkmcnt(2)\n\t"
                   "v_readfirstlane_b32 %3, %2\n\ts_cmp_lg_u32 %3, 0\n\ts_cbranch_scc1 2f\n\ts_sub_u32 s72, s72, 1\n\ts_cbranch_scc0 1b\n2:\n\ts_waitcnt lgkmcnt(0)"
                   : "=&v"(alo), "=&v"(ahi), "=&v"(f_), "=&s"(s_)
                   : "v"(L_FLAG + 4 * F_A), "v"(n < 8 ? af : L_ZERO), "v"(n < 8 ? L_ZERO : af), "i"(LDS_SPIN) : "memory", "scc", "s72");
    }
#pragma unroll
    for (int j = 0; j < 4; ++j) acc[j] = mfma(alo, T[j], f4{0.f, 0.f, 0.f, 0.f});
#pragma unroll
    for (int j = 0; j < 4; ++j) acc[j] = mfma(ahi, T[j + 4], acc[j]);
    const f4 G = gemv_fold(acc, lane);
    st_sc1(v4u{__float_as_uint(G[0]), tag, __float_as_uint(G[1]), tag}, rws, rec);
    st_sc1(v4u{__float_as_uint(G[2]), tag, __float_as_uint(G[3]), tag}, rws, rec + 1024);
    lds_post(F_H2);
    STAMP(5);
  } else {
    const int ds = (c - N_S1) % 3, rs = (c - N_S1) / 3;
    const __amdgpu_buffer_rsrc_t rd = mkrsrc(wd + (size_t)rs * 288 * DM + (3 * ds + w) * 64, 287 * DM * 2 + 128);
    const int voff = rowl * (DM * 2) + chk, voff4 = n >= 8 ? OOB : voff;
    lds_wait<1>(FL + F_ISS, 8);
    v4u R[24];
#pragma unroll
    for (int i = 0; i < 24; ++i) R[i] = ld_nt(rd, voff, (64 * (i >> 3) + (i & 7)) * (DM * 2));
    __builtin_amdgcn_sched_barrier(0);
    STAMP(1);
    lds_wait<1>(FL + F_H, 1);
    STAMP(2);
    const unsigned tag = FL[F_TAG];
    const int ao = L_H + (n & 3) * H_STR + afo;
    gemv_group(R, *(const LAS v4u*)(L + ao), acc);
    __builtin_amdgcn_sched_barrier(0);
#pragma unroll
    for (int i = 0; i < 8; ++i) R[i] = ld_nt(rd, voff, (192 + i) * (DM * 2));
    __builtin_amdgcn_sched_barrier(0);
    gemv_group(R + 8, *(const LAS v4u*)(L + ao + 128), acc);
    __builtin_amdgcn_sched_barrier(0);
#pragma unroll
    for (int i = 0; i < 8; ++i) R[8 + i] = ld_nt(rd, voff4, (256 + i) * (DM * 2));
    __builtin_amdgcn_sched_barrier(0);
    gemv_group(R + 16, *(const LAS v4u*)(L + ao + 256), acc);
    gemv_group(R, *(const LAS v4u*)(L + ao + 384), acc);
    gemv_group(R + 8, *(const LAS v4u*)(L + ao + 512), acc);
    const f4 G = gemv_finish(acc, lane);
    const int rec = WS_F2 + (ds * 16 + rs) * 6144 + w * 2048 + lane * 16;
    st_sc1(v4u{__float_as_uint(G[0]), tag, __float_as_uint(G[1]), tag}, rws, rec);
    st_sc1(v4u{__float_as_uint(G[2]), tag, __float_as_uint(G[3]), tag}, rws, rec + 1024);
    STAMP(5);
  }
  STAMP_FLUSH();
}

extern "C" void kernel_launch(void* const* d_in, const int* in_sizes, int n_in, void* d_out, int out_size, void* d_ws,
                              size_t ws_size, hipStream_t stream) {
  fused_decode<<<NCU, NTHR, 0, stream>>>((const u16*)d_in[0], (const u16*)d_in[1], (const u16*)d_in[2], (const u16*)d_in[3],
                                         (const u16*)d_in[4], (const u16*)d_in[6], (const u16*)d_in[7], (const u16*)d_in[8],
                                         (u16*)d_out, (unsigned char*)d_ws);
}
```

```cpp
#include <hip/hip_runtime.h>
#include <stdint.h>

typedef unsigned short u16;
typedef unsigned v4u __attribute__((ext_vector_type(4)));
typedef unsigned v2u __attribute__((ext_vector_type(2)));
typedef float f4 __attribute__((ext_vector_type(4)));
typedef short s8v __attribute__((ext_vector_type(8)));
typedef __bf16 bf2v __attribute__((ext_vector_type(2)));
#define LAS __attribute__((address_space(3)))
#define GAS __attribute__((address_space(1)))
#define STR2(x) #x
#define STR(x) STR2(x)

constexpr int DM = 576, SK = 8192, WIN = 2048, FF = 4608, NQ = 4096;
constexpr int NCU = 256, NTHR = 768;
constexpr int N_S1 = 208, N_WO = 192, N_FIN = 144;
constexpr int OOB = 0x40000000;
constexpr int NT = 2, SC1 = 16;
constexpr float LOG2E = 1.4426950408889634f, QK_C = 0.08838834764831845f * LOG2E;

#define TAU 1008
#define AHEAD 28
#define DSTEP 10u
#define T_A 1355
#define T_B 1475
#define D_AB 26
#define VSPLIT 10
#define WOCNT 4
#define STAG 6
#define CODEPF 23552

constexpr int S1_ROW = (NQ + 2 * FF) * 4;
constexpr int WS_EPOCH = 0, WS_T0 = 1024, WS_S1 = 4096, WS_H1 = WS_S1 + 4 * S1_ROW, WS_H2 = WS_H1 + NCU * 1024,
              WS_F2 = WS_H2 + 3 * 64 * 6144, WS_END = WS_F2 + 3 * 16 * 6144;
constexpr int XN_STR = 1168, H_STR = 656;
constexpr int L_XN = 0, L_H = L_XN + 4 * XN_STR, L_Q = L_H + 4 * H_STR, L_S1R = L_Q + 512, L_AM = L_S1R + 3 * 1024,
              L_AL = L_AM + 64, L_AD = L_AL + 64, L_AF = L_AD + 8 * 1024, L_XS = L_AF + 1024, L_DUM = L_XS + 5376,
              L_ZERO = L_DUM + 1024, L_FLAG = L_ZERO + 256, L_END = L_FLAG + 64;
enum { F_XN = 0, F_TAG, F_Q, F_H, F_S1, F_ISS, F_D, F_PUB, F_L, F_A, F_H2 };
static_assert(L_H % 16 == 0 && L_Q % 16 == 0 && L_S1R % 16 == 0 && L_AM % 16 == 0 && L_AD % 16 == 0 && L_XS % 16 == 0 &&
              L_DUM % 16 == 0 && L_ZERO % 16 == 0, "align");
#define GLB_N 65536
constexpr unsigned LDS_SPIN = 1u << 22, GLB_SPIN = GLB_N;

#define PACE(kb) asm volatile("1:\n\ts_memrealtime s[92:93]\n\ts_waitcnt lgkmcnt(0)\n\ts_sub_u32 s92, s92, %0\n\ts_cmp_lt_i32 s92, %1\n\t" \
    "s_cbranch_scc0 2f\n\ts_sleep 1\n\ts_branch 1b\n2:" :: "s"(tbase), "i"((int)(((kb) * TAU) >> 8)) : "scc", "memory", "s92", "s93")
#define PACE_A(kb) asm volatile("1:\n\ts_memrealtime s[92:93]\n\ts_waitcnt lgkmcnt(0)\n\ts_sub_u32 s92, s92, %0\n\ts_cmp_lt_i32 s92, %1\n\t" \
    "s_cbranch_scc0 2f\n\ts_sleep 1\n\ts_branch 1b\n2:\n\ts_add_u32 %0, %0, %2" : "+s"(tbase) : "i"((int)(((kb) * TAU) >> 8)), "s"(dstep) : "scc", "memory", "s92", "s93")
#define PLOAD(d, voff, soff) asm volatile("buffer_load_dwordx4 %0, %1, %2, %3 offen sc1" : "=v"(d) : "v"(voff), "s"(dws), "s"(soff) : "memory")
#define EXIT_WAVE { __builtin_amdgcn_endpgm(); __builtin_unreachable(); }

__device__ __forceinline__ __amdgpu_buffer_rsrc_t mkrsrc(const void* p, unsigned bytes) {
  return __builtin_amdgcn_make_buffer_rsrc((void*)p, 0, bytes, 0x00020000);
}
__device__ __forceinline__ v4u ld_nt(__amdgpu_buffer_rsrc_t r, int voff, int soff = 0) {
  return __builtin_amdgcn_raw_buffer_load_b128(r, voff, soff, NT);
}
__device__ __forceinline__ void st_sc1(v4u v, __amdgpu_buffer_rsrc_t r, int voff) {
  __builtin_amdgcn_raw_buffer_store_b128(v, r, voff, 0, SC1);
}
__device__ __forceinline__ f4 mfma(v4u a, v4u b, f4 c) {
  return __builtin_amdgcn_mfma_f32_16x16x32_bf16(__builtin_bit_cast(s8v, a), __builtin_bit_cast(s8v, b), c, 0, 0, 0);
}
__device__ __forceinline__ unsigned cvtpk(float lo, float hi) {
  bf2v r = {(__bf16)lo, (__bf16)hi};
  return __builtin_bit_cast(unsigned, r);
}
__device__ __forceinline__ float rbf(float f) { return (float)(__bf16)f; }
__device__ __forceinline__ float rcp(float f) { return __builtin_amdgcn_rcpf(f); }
__device__ __forceinline__ float ex2(float f) { return __builtin_amdgcn_exp2f(f); }
__device__ __forceinline__ float blo(unsigned u) { return __uint_as_float(u << 16); }
__device__ __forceinline__ float bhi(unsigned u) { return __uint_as_float(u & 0xffff0000u); }
__device__ __forceinline__ unsigned mktag(unsigned e) { return ((e + 1u) & 0xffffu) ? e + 1u : e + 2u; }
template <int CTRL> __device__ __forceinline__ float dpp(float x) {
  return __builtin_bit_cast(float, __builtin_amdgcn_mov_dpp(__builtin_bit_cast(int, x), CTRL, 0xf, 0xf, true));
}
constexpr int ROR1 = 0x121, ROR2 = 0x122, ROR4 = 0x124, ROR8 = 0x128, XOR1 = 0xB1;
__device__ __forceinline__ float row_sum(float x) { x += dpp<ROR8>(x); x += dpp<ROR4>(x); x += dpp<ROR2>(x); x += dpp<ROR1>(x); return x; }
__device__ __forceinline__ float row_max(float x) {
  x = fmaxf(x, dpp<ROR8>(x)); x = fmaxf(x, dpp<ROR4>(x)); x = fmaxf(x, dpp<ROR2>(x)); x = fmaxf(x, dpp<ROR1>(x)); return x; }
__device__ __forceinline__ void swap16(float& a, float& b) {
  auto s = __builtin_amdgcn_permlane16_swap(__float_as_uint(a), __float_as_uint(b), false, false);
  a = __uint_as_float(s[0]); b = __uint_as_float(s[1]);
}
__device__ __forceinline__ void swap32(float& a, float& b) {
  auto s = __builtin_amdgcn_permlane32_swap(__float_as_uint(a), __float_as_uint(b), false, false);
  a = __uint_as_float(s[0]); b = __uint_as_float(s[1]);
}
__device__ __forceinline__ float xrow_sum(float x) { float a = x, b = x; swap16(a, b); x = a + b; a = x; b = x; swap32(a, b); return a + b; }
__device__ __forceinline__ float xrow_max(float x) { float a = x, b = x; swap16(a, b); x = fmaxf(a, b); a = x; b = x; swap32(a, b); return fmaxf(a, b); }
__device__ __forceinline__ float pair_sum(float a, float b) {
  a += dpp<ROR8>(a); b += dpp<ROR8>(b);
  swap16(a, b);
  float t = a + b, u = t;
  swap32(t, u);
  return t + u;
}

template <int SLEEP> __device__ __forceinline__ void lds_wait(volatile LAS unsigned* f, unsigned want) {
#pragma nounroll
  for (unsigned s = 0; s < LDS_SPIN; ++s) {
    if ((unsigned)__builtin_amdgcn_readfirstlane(*f) >= want) break;
    if (SLEEP) __builtin_amdgcn_s_sleep(SLEEP);
  }
  asm volatile("" ::: "memory");
}
__device__ __forceinline__ void wait_until(int ticks, unsigned t0) {
  asm volatile("1:\n\ts_memrealtime s[92:93]\n\ts_waitcnt lgkmcnt(0)\n\ts_sub_u32 s92, s92, %0\n\ts_cmp_lt_i32 s92, %1\n\ts_cbranch_scc1 1b"
               :: "s"(t0), "s"(ticks) : "scc", "memory", "s92", "s93");
}
__device__ __forceinline__ void lds_post(int flag) {
  asm volatile("s_mov_b64 exec, 1\n\tds_add_u32 %0, %1\n\ts_mov_b64 exec, -1" :: "v"(L_FLAG + 4 * flag), "v"(1u) : "memory");
}
__device__ __forceinline__ unsigned lds_arrive(int flag) {
  unsigned o, r;
  asm volatile("s_mov_b64 exec, 1\n\tds_add_rtn_u32 %0, %2, %3\n\ts_mov_b64 exec, -1\n\ts_waitcnt lgkmcnt(0)\n\tv_readfirstlane_b32 %1, %0"
               : "=&v"(o), "=s"(r) : "v"(L_FLAG + 4 * flag), "v"(1u) : "memory");
  return r;
}

__device__ __forceinline__ void gemv_group(const v4u* r, v4u a, f4* acc) {
#pragma unroll
  for (int w = 0; w < 4; ++w) {
    v4u lo, hi;
#pragma unroll
    for (int p = 0; p < 4; ++p) {
      lo[p] = __builtin_amdgcn_perm(r[2 * p + 1][w], r[2 * p][w], 0x05040100u);
      hi[p] = __builtin_amdgcn_perm(r[2 * p + 1][w], r[2 * p][w], 0x07060302u);
    }
    acc[2 * w] = mfma(a, lo, acc[2 * w]);
    acc[2 * w + 1] = mfma(a, hi, acc[2 * w + 1]);
  }
}
__device__ __forceinline__ f4 gemv_fold(const f4* e, int lane) {
  f4 G;
#pragma unroll
  for (int j = 0; j < 4; ++j) {
    float e0 = e[j][0], e1 = e[j][1], e2 = e[j][2], e3 = e[j][3];
    swap16(e0, e1); swap16(e2, e3);
    const float t0 = e0 + dpp<ROR8>(e1), t1 = dpp<ROR8>(e2) + e3;
    G[j] = (lane & 8) ? t1 : t0;
  }
  return G;
}
__device__ __forceinline__ f4 gemv_finish(const f4* acc, int lane) {
  f4 e[4];
#pragma unroll
  for (int j = 0; j < 4; ++j) e[j] = lane < 32 ? acc[j] : acc[j + 4];
  return gemv_fold(e, lane);
}

__global__ void __launch_bounds__(NTHR, 3)
fused_decode(const u16* __restrict__ x, const u16* __restrict__ kc, const u16* __restrict__ vc, const u16* __restrict__ wq,
             const u16* __restrict__ wo, const u16* __restrict__ wg, const u16* __restrict__ wu, const u16* __restrict__ wd,
             u16* __restrict__ out, unsigned char* ws) {
  __shared__ __attribute__((aligned(16))) unsigned char lds_raw[L_END];
  LAS unsigned char* L = (LAS unsigned char*)lds_raw;
  volatile LAS unsigned* FL = (volatile LAS unsigned*)(L + L_FLAG);
  const int c = blockIdx.x;
  const bool isS = c < N_S1;
  const int pair = (c & 7) * 8 + (c >> 5), sp = (c >> 3) & 3;
  int ksl = (c & 7) * 8 + (c >> 3) / 3, cs = (c >> 3) % 3;
  asm volatile("" : "+s"(ksl), "+s"(cs));
  *(volatile LAS unsigned*)(L + L_ZERO + min(threadIdx.x, 79u) * 4) = 0u;
  unsigned long long tstart64;
  asm volatile("s_memrealtime %0\n\ts_waitcnt lgkmcnt(0)\n\ts_barrier" : "=&s"(tstart64)
               : "s"(x), "s"(kc), "s"(vc), "s"(wq), "s"(wo), "s"(wg), "s"(wu), "s"(wd), "s"(out), "s"(ws) : "memory");
  const unsigned tstart = (unsigned)tstart64;
  const int wave = __builtin_amdgcn_readfirstlane(threadIdx.x >> 6);
#define LANE_IDS int lane_ = threadIdx.x & 63; asm volatile("" : "+v"(lane_)); const int lane = lane_, g = lane >> 4, n = lane & 15;
  const __amdgpu_buffer_rsrc_t rws = mkrsrc(ws, WS_END);
  if (wave == 3) {
    const v4u dx = {(unsigned)(uintptr_t)x, (unsigned)((uintptr_t)x >> 32), 4u * DM * 2u, 0x00020000u};
    const v4u de = {(unsigned)(uintptr_t)ws, (unsigned)((uintptr_t)ws >> 32), 4u, 0x00020000u};
    const int xo = (threadIdx.x & 63) * 16, z = 0;
    unsigned keep;
    asm volatile("s_mov_b32 %0, m0\n\ts_mov_b32 m0, %4\n\ts_nop 0\n\t"
                 "buffer_load_dwordx4 %1, %2, 0 offen lds\n\tbuffer_load_dwordx4 %1, %2, 0 offen offset:1024 lds\n\t"
                 "buffer_load_dwordx4 %1, %2, 0 offen offset:2048 lds\n\tbuffer_load_dwordx4 %1, %2, 0 offen offset:3072 lds\n\t"
                 "s_mov_b32 m0, %5\n\ts_movk_i32 s92, 0x1000\n\tbuffer_load_dwordx4 %1, %2, s92 offen lds\n\t"
                 "s_mov_b32 m0, %6\n\ts_nop 0\n\tbuffer_load_dword %7, %3, 0 offen sc1 lds\n\ts_mov_b32 m0, %0"
                 : "=&s"(keep) : "v"(xo), "s"(dx), "s"(de), "i"(L_XS), "i"(L_XS + 4096), "i"(L_XS + 5120), "v"(z) : "memory", "s92");
  }
  if (wave == 11) {
    const int po = (c >> 3) * 1024 + (threadIdx.x & 63) * 16;
    unsigned keep;
    asm volatile("s_mov_b32 %0, m0\n\ts_getpc_b64 s[92:93]\n\ts_and_b32 s92, s92, 0xffffff00\n\ts_mov_b32 s94, %2\n\ts_mov_b32 s95, 0x20000\n\t"
                 "s_mov_b32 m0, %3\n\ts_nop 0\n\tbuffer_load_dwordx4 %1, s[92:95], 0 offen lds\n\ts_mov_b32 m0, %0"
                 : "=&s"(keep) : "v"(po), "i"(CODEPF), "i"(L_DUM) : "memory", "scc", "s92", "s93", "s94", "s95");
  }

  if (__builtin_expect(wave >= 4, 0)) {
    LANE_IDS
    const int w = wave - 4;
    const size_t kvo = ((size_t)pair * SK + (SK - WIN) + sp * 512 + ((w + pair) & 7) * 64) * 128;
    const __amdgpu_buffer_rsrc_t rk = mkrsrc(kc + kvo, 16384), rv = mkrsrc(vc + kvo, 16384);
    const int kvo0 = (n >> 2) * 4096 + (n >> 1) * 256 + (n & 1) * 64 + g * 16, vvo0 = g * 4096 + n * 16;
    unsigned tbase = tstart + (unsigned)(((((isS ? 72 : 24) - AHEAD) + 2 * w) * TAU) >> 8);
    const unsigned dstep = isS ? 0u : DSTEP;
    v4u K[16], V[16];
#pragma unroll
    for (int i = 0; i < 16; ++i) {
      if ((i & 1) == 0) PACE_A(8 * i);
      K[i] = ld_nt(rk, (kvo0 ^ ((i >> 2) * 4096)) + ((i >> 1) & 1) * 2048 + (i & 1) * 128);
    }
#define V_LOAD(i) { if (((i) & 1) == 0) PACE_A(128 + 8 * (i)); V[i] = ld_nt(rv, (vvo0 ^ (((i) >> 2) * 4096)) + (((i) & 1) * 8 + ((i) >> 1)) * 256); }
#pragma unroll
    for (int i = 0; i < VSPLIT; ++i) V_LOAD(i)
    __builtin_amdgcn_sched_barrier(0);
    float m0, m1; unsigned pk;
    auto scores = [&]() __attribute__((always_inline)) {
      f4 E0 = f4{0.f, 0.f, 0.f, 0.f}, E1 = f4{0.f, 0.f, 0.f, 0.f};
      const int qa = L_Q + ((n >> 1) & 1) * 256 + (n & 1) * 64 + g * 16;
#pragma unroll
      for (int tp = 0; tp < 4; ++tp) {
        const int ad = (n >> 2) == tp ? qa : L_ZERO;
        const v4u q0 = *(const LAS v4u*)(L + ad), q1 = *(const LAS v4u*)(L + ad + 128);
        E0 = mfma(q0, K[4 * tp], E0); E1 = mfma(q0, K[4 * tp + 2], E1);
        E0 = mfma(q1, K[4 * tp + 1], E0); E1 = mfma(q1, K[4 * tp + 3], E1);
      }
      const float s0 = ((n & 1) ? E1[1] : E0[0]) + dpp<XOR1>((n & 1) ? E0[1] : E1[0]);
      const float s1 = ((n & 1) ? E1[3] : E0[2]) + dpp<XOR1>((n & 1) ? E0[3] : E1[2]);
      m0 = xrow_max(row_max(s0)); m1 = xrow_max(row_max(s1));
      const float p0 = ex2((s0 - m0) * QK_C), p1 = ex2((s1 - m1) * QK_C);
      const float l0 = xrow_sum(row_sum(p0)), l1 = xrow_sum(row_sum(p1));
      if (lane == 0) {
        *(LAS float*)(L + L_AM + w * 4) = m0; *(LAS float*)(L + L_AM + 32 + w * 4) = m1;
        *(LAS float*)(L + L_AL + w * 4) = l0; *(LAS float*)(L + L_AL + 32 + w * 4) = l1;
      }
      lds_post(F_L);
      pk = cvtpk(p0, p1);
    };
    const bool early = __builtin_amdgcn_readfirstlane(FL[F_Q]) != 0;
    asm volatile("" ::: "memory");
    if (early) scores();
    __builtin_amdgcn_sched_barrier(0);
#pragma unroll
    for (int i = VSPLIT; i < 16; ++i) V_LOAD(i)
    __builtin_amdgcn_sched_barrier(0);
    lds_post(F_ISS);
    if (!early) { lds_wait<1>(FL + F_Q, 1); scores(); }
    lds_wait<1>(FL + F_L, 8);
    float scale; unsigned t1, t3;
    {
      const int h = g >> 1;
      const f4 ma = *(const LAS f4*)(L + L_AM + h * 32), mb = *(const LAS f4*)(L + L_AM + h * 32 + 16);
      const f4 la = *(const LAS f4*)(L + L_AL + h * 32), lb = *(const LAS f4*)(L + L_AL + h * 32 + 16);
      const float Mh = fmaxf(fmaxf(fmaxf(ma[0], ma[1]), fmaxf(ma[2], ma[3])), fmaxf(fmaxf(mb[0], mb[1]), fmaxf(mb[2], mb[3])));
      float Lh = 0.f;
#pragma unroll
      for (int k = 0; k < 4; ++k) Lh = fmaf(la[k], ex2((ma[k] - Mh) * QK_C), Lh);
#pragma unroll
      for (int k = 0; k < 4; ++k) Lh = fmaf(lb[k], ex2((mb[k] - Mh) * QK_C), Lh);
      scale = ex2(((h ? m1 : m0) - Mh) * QK_C) * rcp(Lh);
      const unsigned th = FL[F_TAG] << 16, ml = __float_as_uint((g & 1) ? Lh : Mh);
      t1 = th | (ml & 0xffffu); t3 = th | (ml >> 16);
    }
    int oh = WS_H1 + (pair * 4 + sp) * 1024 + (g >> 1) * 512 + n * 32 + (g & 1) * 16, la_ = L_AD + lane * 16;
    const unsigned hs = (n & 8) ? 0x0302u : 0x0100u, one = (n & 1) ? ((hs << 16) | 0x0c0cu) : (0x0c0c0000u | hs);
    unsigned sel[4];
#pragma unroll
    for (int r = 0; r < 4; ++r) sel[r] = ((n & 7) >> 1) == r ? one : 0x0c0c0c0cu;
    asm volatile("" : "+v"(scale), "+v"(t1), "+v"(t3), "+v"(oh), "+v"(la_));
    __builtin_amdgcn_sched_barrier(0);
    f4 O = f4{0.f, 0.f, 0.f, 0.f};
#define PV_STEP(i) { const unsigned pb = (unsigned)__builtin_amdgcn_mov_dpp((int)pk, 0x150 + (i), 0xf, 0xf, true); \
      v4u a; a[0] = __builtin_amdgcn_perm(pb, pb, sel[0]); a[1] = __builtin_amdgcn_perm(pb, pb, sel[1]); \
      a[2] = __builtin_amdgcn_perm(pb, pb, sel[2]); a[3] = __builtin_amdgcn_perm(pb, pb, sel[3]); O = mfma(a, V[i], O); }
    PV_STEP(0) PV_STEP(1) PV_STEP(2) PV_STEP(3) PV_STEP(4) PV_STEP(5) PV_STEP(6) PV_STEP(7)
    PV_STEP(8) PV_STEP(9) PV_STEP(10) PV_STEP(11) PV_STEP(12) PV_STEP(13) PV_STEP(14) PV_STEP(15)
    *(LAS f4*)(L + la_ + w * 1024) = O * scale;
    if (lds_arrive(F_D) == 7) {
      f4 t[8];
#pragma unroll
      for (int k = 0; k < 8; ++k) t[k] = *(const LAS f4*)(L + la_ + k * 1024);
      const f4 o = ((t[0] + t[1]) + (t[2] + t[3])) + ((t[4] + t[5]) + (t[6] + t[7]));
      st_sc1(v4u{cvtpk(o[0], o[1]), t1, cvtpk(o[2], o[3]), t3}, rws, oh);
      FL[F_PUB] = 1;
    }
    EXIT_WAVE
  }

  if (__builtin_expect(wave == 3, 0)) {
    __builtin_amdgcn_s_setprio(3);
    LANE_IDS
    const v4u dws = {(unsigned)(uintptr_t)ws, (unsigned)((uintptr_t)ws >> 32), (unsigned)WS_END, 0x00020000u};
    const int zero = 0;
    asm volatile("s_waitcnt vmcnt(0)" ::: "memory");
    const unsigned tag = mktag(*(const volatile LAS unsigned*)(L + L_XS + 5120));
    st_sc1(v4u{tstart, tag, tstart, tag}, rws, (c < 8 && lane < 16) ? WS_T0 + lane * 128 + c * 16 : OOB);
    if (isS) {
      float xf[40], s = 0.f;
#pragma unroll
      for (int i = 0; i < 5; ++i) {
        const v4u X = *(const volatile LAS v4u*)(L + ((i == 4 && n >= 8) ? L_ZERO : L_XS + g * (DM * 2) + (n + 16 * i) * 16));
#pragma unroll
        for (int k = 0; k < 4; ++k) { xf[i * 8 + 2 * k] = blo(X[k]); xf[i * 8 + 2 * k + 1] = bhi(X[k]); }
      }
#pragma unroll
      for (int i = 0; i < 40; ++i) s += xf[i];
      const float mean = row_sum(s) * (1.f / DM);
      float v = 0.f;
#pragma unroll
      for (int i = 0; i < 40; ++i) { const float d = (i >= 32 && n >= 8) ? 0.f : xf[i] - mean; v = fmaf(d, d, v); }
      const float var = row_sum(v) * (1.f / DM);
      const float mb = rbf(mean), rden = rcp(rbf(__builtin_amdgcn_sqrtf(rbf(rbf(var) + 1e-5f))));
#pragma unroll
      for (int i = 0; i < 5; ++i) {
        v4u o;
#pragma unroll
        for (int k = 0; k < 4; ++k) o[k] = cvtpk(rbf(xf[i * 8 + 2 * k] - mb) * rden, rbf(xf[i * 8 + 2 * k + 1] - mb) * rden);
        if (i < 4 || n < 8) *(LAS v4u*)(L + L_XN + g * XN_STR + (n + 16 * i) * 16) = o;
      }
    }
    FL[F_TAG] = tag;
    asm volatile("" ::: "memory");
    FL[F_XN] = 1;
    unsigned t0;
    {
      const int off = WS_S1 + (pair >> 4) * S1_ROW + (pair & 15) * 1024 + lane * 16;
      const int offt = WS_T0 + (c & 15) * 128 + (lane & 7) * 16;
      v4u r, t;
#pragma nounroll
      for (unsigned s = 0; s < GLB_SPIN; ++s) {
        PLOAD(r, off, zero); PLOAD(t, offt, zero);
        asm volatile("s_waitcnt vmcnt(0)" : "+v"(r), "+v"(t) :: "memory");
        if (__all((r[1] == tag) & (r[3] == tag) & (t[1] == tag) & (t[3] == tag))) break;
      }
      float late = (float)(int)(t[0] - tstart);
      late = fmaxf(late, dpp<ROR4>(late)); late = fmaxf(late, dpp<ROR2>(late)); late = fmaxf(late, dpp<ROR1>(late));
      t0 = tstart + (unsigned)__builtin_amdgcn_readfirstlane((int)late);
      *(LAS v2u*)(L + L_Q + lane * 8) = v2u{r[0], r[2]};
      asm volatile("" ::: "memory");
      FL[F_Q] = 1;
    }
    if (!isS) {
      const int rs = (c - N_S1) / 3, up = FF * 4;
      v4u G[5], U[5];
      int off[5], loff[5];
#pragma unroll
      for (int i = 0; i < 5; ++i) {
        const int t = min(lane + 64 * i, 287), b = t / 72, qd = t - b * 72;
        off[i] = WS_S1 + b * S1_ROW + (NQ + 288 * rs + 4 * qd) * 4; loff[i] = b * H_STR + qd * 8;
      }
#pragma nounroll
      for (unsigned s = 0; s < GLB_SPIN; ++s) {
#pragma unroll
        for (int i = 0; i < 5; ++i) { PLOAD(G[i], off[i], zero); PLOAD(U[i], off[i], up); }
        asm volatile("s_waitcnt vmcnt(0)" : "+v"(G[0]), "+v"(G[1]), "+v"(G[2]), "+v"(G[3]), "+v"(G[4]),
                     "+v"(U[0]), "+v"(U[1]), "+v"(U[2]), "+v"(U[3]), "+v"(U[4]) :: "memory");
        bool ok = true;
#pragma unroll
        for (int i = 0; i < 5; ++i) ok &= (G[i][1] == tag) & (G[i][3] == tag) & (U[i][1] == tag) & (U[i][3] == tag);
        if (__all(ok)) break;
        __builtin_amdgcn_s_sleep(4);
      }
      if (lane < 32) *(LAS v2u*)(L + L_H + (lane >> 3) * H_STR + 576 + (lane & 7) * 8) = v2u{0u, 0u};
#pragma unroll
      for (int i = 0; i < 5; ++i) {
        float hv[4];
#pragma unroll
        for (int k = 0; k < 4; ++k) {
          const unsigned gw = G[i][(k >> 1) * 2], uw = U[i][(k >> 1) * 2];
          const float a = (k & 1) ? bhi(gw) : blo(gw), u = (k & 1) ? bhi(uw) : blo(uw);
          const float z2 = (-2.f * 0.7978845608028654f * LOG2E) * (a + 0.044715f * a * a * a);
          hv[k] = rbf(a * rcp(1.f + ex2(z2))) * u;
        }
        *(LAS v2u*)(L + L_H + loff[i]) = v2u{cvtpk(hv[0], hv[1]), cvtpk(hv[2], hv[3])};
      }
      asm volatile("" ::: "memory");
      FL[F_H] = 1;
    }
    if (c >= N_WO) EXIT_WAVE
    const int fcs = c / 48, fr = c - fcs * 48, fw = fr >> 4, fpc = (fr >> 3) & 1, fgr = fr & 7;
    int oo = ((2 * (fgr & 1) + ((fgr >> 1) & 1)) * DM + 64 * (3 * fcs + fw) + 8 * (lane & 7) + 4 * (fgr >> 2) + 2 * fpc + (g & 1)) * 2;
    oo = (c < N_FIN && (lane & 40) == 0) ? oo : OOB;
    float xres = blo(__builtin_amdgcn_raw_buffer_load_b16(mkrsrc(x, 4 * DM * 2), oo, 0, 0));
    asm volatile("" : "+v"(xres), "+v"(oo));
    {
      const int hh = ksl >> 1, pc = (((n & 3) * 16 + (hh >> 1)) * 4 + 2 * (n >> 3));
      const int o1 = WS_H1 + pc * 1024 + (hh & 1) * 512 + (8 * (ksl & 1) + 4 * ((n >> 2) & 1) + g) * 32;
      unsigned p[12];
#pragma unroll
      for (int k = 0; k < 12; ++k) p[k] = 0u;
      lds_wait<1>(FL + F_PUB, 1);
      wait_until(max((int)((unsigned)__builtin_amdgcn_s_memrealtime() - t0) + D_AB, T_A), t0);
      asm volatile(
        "s_mov_b64 s[70:71], 0\n\t" "s_mov_b32 s72, %[n]\n\t"
        "v_cndmask_b32_e64 v119, %[o1], 2.0, s[70:71]\n\t"
        "buffer_load_dwordx4 v[120:123], v119, %[d], 0 offen offset:0 sc1\n\t"
        "buffer_load_dwordx4 v[124:127], v119, %[d], 0 offen offset:16 sc1\n\t"
        "buffer_load_dwordx4 v[128:131], v119, %[d], 0 offen offset:1024 sc1\n\t"
        "buffer_load_dwordx4 v[132:135], v119, %[d], 0 offen offset:1040 sc1\n\t"
        "s_sleep " STR(STAG) "\n\t"
        "1:\n\t"
        "v_cndmask_b32_e64 v119, %[o1], 2.0, s[70:71]\n\t"
        "buffer_load_dwordx4 v[136:139], v119, %[d], 0 offen offset:0 sc1\n\t"
        "buffer_load_dwordx4 v[140:143], v119, %[d], 0 offen offset:16 sc1\n\t"
        "buffer_load_dwordx4 v[144:147], v119, %[d], 0 offen offset:1024 sc1\n\t"
        "buffer_load_dwordx4 v[148:151], v119, %[d], 0 offen offset:1040 sc1\n\t"
        "s_waitcnt vmcnt(4)\n\t"
        "v_bitop3_b32 v112, v121, v123, %[t] bitop3:0x7e\n\t"
        "v_bitop3_b32 v113, v125, v127, %[t] bitop3:0x7e\n\t"
        "v_bitop3_b32 v114, v129, v131, %[t] bitop3:0x7e\n\t"
        "v_bitop3_b32 v115, v133, v135, %[t] bitop3:0x7e\n\t"
        "v_or3_b32 v112, v112, v113, v114\n\t" "v_or_b32 v112, v112, v115\n\t" "v_cmp_gt_u32 vcc, 0x10000, v112\n\t"
        "v_perm_b32 v113, v123, v121, %[sl]\n\t"
        "v_perm_b32 v114, v127, v125, %[sl]\n\t"
        "v_perm_b32 v115, v131, v129, %[sl]\n\t"
        "v_perm_b32 v116, v135, v133, %[sl]\n\t"
        "s_or_b64 s[70:71], s[70:71], vcc\n\t"
        "v_cndmask_b32 %[p0], %[p0], v120, vcc\n\t"
        "v_cndmask_b32 %[p1], %[p1], v122, vcc\n\t"
        "v_cndmask_b32 %[p2], %[p2], v124, vcc\n\t"
        "v_cndmask_b32 %[p3], %[p3], v126, vcc\n\t"
        "v_cndmask_b32 %[p4], %[p4], v128, vcc\n\t"
        "v_cndmask_b32 %[p5], %[p5], v130, vcc\n\t"
        "v_cndmask_b32 %[p6], %[p6], v132, vcc\n\t"
        "v_cndmask_b32 %[p7], %[p7], v134, vcc\n\t"
        "v_cndmask_b32 %[p8], %[p8], v113, vcc\n\t"
        "v_cndmask_b32 %[p9], %[p9], v114, vcc\n\t"
        "v_cndmask_b32 %[p10], %[p10], v115, vcc\n\t"
        "v_cndmask_b32 %[p11], %[p11], v116, vcc\n\t"
        "s_cmp_eq_u64 s[70:71], exec\n\t" "s_cbranch_scc1 2f\n\t"
        "v_cndmask_b32_e64 v119, %[o1], 2.0, s[70:71]\n\t"
        "buffer_load_dwordx4 v[120:123], v119, %[d], 0 offen offset:0 sc1\n\t"
        "buffer_load_dwordx4 v[124:127], v119, %[d], 0 offen offset:16 sc1\n\t"
        "buffer_load_dwordx4 v[128:131], v119, %[d], 0 offen offset:1024 sc1\n\t"
        "buffer_load_dwordx4 v[132:135], v119, %[d], 0 offen offset:1040 sc1\n\t"
        "s_waitcnt vmcnt(4)\n\t"
        "v_bitop3_b32 v112, v137, v139, %[t] bitop3:0x7e\n\t"
        "v_bitop3_b32 v113, v141, v143, %[t] bitop3:0x7e\n\t"
        "v_bitop3_b32 v114, v145, v147, %[t] bitop3:0x7e\n\t"
        "v_bitop3_b32 v115, v149, v151, %[t] bitop3:0x7e\n\t"
        "v_or3_b32 v112, v112, v113, v114\n\t" "v_or_b32 v112, v112, v115\n\t" "v_cmp_gt_u32 vcc, 0x10000, v112\n\t"
        "v_perm_b32 v113, v139, v137, %[sl]\n\t"
        "v_perm_b32 v114, v143, v141, %[sl]\n\t"
        "v_perm_b32 v115, v147, v145, %[sl]\n\t"
        "v_perm_b32 v116, v151, v149, %[sl]\n\t"
        "s_or_b64 s[70:71], s[70:71], vcc\n\t"
        "v_cndmask_b32 %[p0], %[p0], v136, vcc\n\t"
        "v_cndmask_b32 %[p1], %[p1], v138, vcc\n\t"
        "v_cndmask_b32 %[p2], %[p2], v140, vcc\n\t"
        "v_cndmask_b32 %[p3], %[p3], v142, vcc\n\t"
        "v_cndmask_b32 %[p4], %[p4], v144, vcc\n\t"
        "v_cndmask_b32 %[p5], %[p5], v146, vcc\n\t"
        "v_cndmask_b32 %[p6], %[p6], v148, vcc\n\t"
        "v_cndmask_b32 %[p7], %[p7], v150, vcc\n\t"
        "v_cndmask_b32 %[p8], %[p8], v113, vcc\n\t"
        "v_cndmask_b32 %[p9], %[p9], v114, vcc\n\t"
        "v_cndmask_b32 %[p10], %[p10], v115, vcc\n\t"
        "v_cndmask_b32 %[p11], %[p11], v116, vcc\n\t"
        "s_cmp_eq_u64 s[70:71], exec\n\t" "s_cbranch_scc1 2f\n\t"
        "s_bcnt0_i32_b64 s73, s[70:71]\n\t" "s_cmp_lt_u32 s73, 17\n\t" "s_cbranch_scc1 3f\n\t" "s_sleep 12\n\t" "3:\n\t"
        "s_sub_u32 s72, s72, 1\n\t" "s_cbranch_scc0 1b\n\t"
        "2:\n\t" "s_nop 1 ; POLL_END"
        : [p0] "+v"(p[0]), [p1] "+v"(p[1]), [p2] "+v"(p[2]), [p3] "+v"(p[3]), [p4] "+v"(p[4]), [p5] "+v"(p[5]), [p6] "+v"(p[6]), [p7] "+v"(p[7]), [p8] "+v"(p[8]), [p9] "+v"(p[9]), [p10] "+v"(p[10]), [p11] "+v"(p[11])
        : [o1] "v"(o1), [d] "s"(dws), [t] "v"(tag << 16), [sl] "s"(0x05040100u), [n] "i"(GLB_SPIN)
        : "memory", "vcc", "scc", "s70", "s71", "s72", "s73", "v112", "v113", "v114", "v115", "v116", "v117", "v118", "v119", "v120", "v121", "v122", "v123", "v124", "v125", "v126", "v127", "v128", "v129", "v130", "v131", "v132", "v133", "v134", "v135", "v136", "v137", "v138", "v139", "v140", "v141", "v142", "v143", "v144", "v145", "v146", "v147", "v148", "v149", "v150", "v151");
      const float ma = __uint_as_float(p[8]), mb = __uint_as_float(p[10]);
      float M = fmaxf(ma, mb); M = fmaxf(M, dpp<ROR8>(M));
      const float ea = __uint_as_float(p[9]) * ex2((ma - M) * QK_C), eb = __uint_as_float(p[11]) * ex2((mb - M) * QK_C);
      float Ls = ea + eb; Ls += dpp<ROR8>(Ls);
      const float inv = rcp(Ls);
      v4u a;
#pragma unroll
      for (int k = 0; k < 4; ++k) {
        float lo = fmaf(ea, blo(p[k]), eb * blo(p[4 + k])), hi = fmaf(ea, bhi(p[k]), eb * bhi(p[4 + k]));
        lo += dpp<ROR8>(lo); hi += dpp<ROR8>(hi);
        a[k] = cvtpk(lo * inv, hi * inv);
      }
      *(LAS v4u*)(L + L_AF + lane * 16) = a;
      asm volatile("" ::: "memory");
      FL[F_A] = 1;
      asm volatile("s_waitcnt vmcnt(0) ; POLL_DRAIN" ::: "memory");
    }
    if (c >= N_FIN) EXIT_WAVE
    const int lo_ = fw * 2048 + fpc * 1024 + fgr * 128 + (lane & 7) * 16;
    int o[9]; float pa[8], pb[8], f0 = 0.f, f1 = 0.f;
    o[8] = WS_F2 + (fcs * 16 + (lane >> 3)) * 6144 + lo_;
#pragma unroll
    for (int i = 0; i < 8; ++i) { o[i] = WS_H2 + (fcs * 64 + 8 * i + (lane >> 3)) * 6144 + lo_; pa[i] = 0.f; pb[i] = 0.f; }
    const __amdgpu_buffer_rsrc_t rout = mkrsrc(out, 4 * DM * 2);
    int oe = (c | lane) ? OOB : WS_EPOCH;
    asm volatile("" : "+v"(oe));
    lds_wait<0>(FL + F_H2, 3);
    wait_until(max((int)((unsigned)__builtin_amdgcn_s_memrealtime() - t0) + D_AB, T_B), t0);
    asm volatile(
        "s_mov_b32 s72, " STR(GLB_N) "\n\t" "s_mov_b32 s73, 0xc000\n\t"
        "buffer_load_dwordx4 v[88:91], %[o0], %[d], 0 offen sc1\n\t"
        "buffer_load_dwordx4 v[92:95], %[o1], %[d], 0 offen sc1\n\t"
        "buffer_load_dwordx4 v[96:99], %[o2], %[d], 0 offen sc1\n\t"
        "buffer_load_dwordx4 v[100:103], %[o3], %[d], 0 offen sc1\n\t"
        "buffer_load_dwordx4 v[104:107], %[o4], %[d], 0 offen sc1\n\t"
        "buffer_load_dwordx4 v[108:111], %[o5], %[d], 0 offen sc1\n\t"
        "buffer_load_dwordx4 v[112:115], %[o6], %[d], 0 offen sc1\n\t"
        "buffer_load_dwordx4 v[116:119], %[o7], %[d], 0 offen sc1\n\t"
        "buffer_load_dwordx4 v[120:123], %[o8], %[d], 0 offen sc1\n\t"
        "buffer_load_dwordx4 v[124:127], %[o8], %[d], s73 offen sc1\n\t"
        "s_sleep " STR(STAG) "\n\t"
        "1:\n\t"
        "buffer_load_dwordx4 v[128:131], %[o0], %[d], 0 offen sc1\n\t"
        "buffer_load_dwordx4 v[132:135], %[o1], %[d], 0 offen sc1\n\t"
        "buffer_load_dwordx4 v[136:139], %[o2], %[d], 0 offen sc1\n\t"
        "buffer_load_dwordx4 v[140:143], %[o3], %[d], 0 offen sc1\n\t"
        "buffer_load_dwordx4 v[144:147], %[o4], %[d], 0 offen sc1\n\t"
        "buffer_load_dwordx4 v[148:151], %[o5], %[d], 0 offen sc1\n\t"
        "buffer_load_dwordx4 v[152:155], %[o6], %[d], 0 offen sc1\n\t"
        "buffer_load_dwordx4 v[156:159], %[o7], %[d], 0 offen sc1\n\t"
        "buffer_load_dwordx4 v[160:163], %[o8], %[d], 0 offen sc1\n\t"
        "buffer_load_dwordx4 v[164:167], %[o8], %[d], s73 offen sc1\n\t"
        "s_waitcnt vmcnt(10)\n\t"
        "v_bitop3_b32 v78, v89, v91, %[t] bitop3:0x7e\n\t"
        "v_bitop3_b32 v79, v93, v95, %[t] bitop3:0x7e\n\t"
        "v_bitop3_b32 v80, v97, v99, %[t] bitop3:0x7e\n\t"
        "v_bitop3_b32 v81, v101, v103, %[t] bitop3:0x7e\n\t"
        "v_bitop3_b32 v82, v105, v107, %[t] bitop3:0x7e\n\t"
        "v_bitop3_b32 v83, v109, v111, %[t] bitop3:0x7e\n\t"
        "v_bitop3_b32 v84, v113, v115, %[t] bitop3:0x7e\n\t"
        "v_bitop3_b32 v85, v117, v119, %[t] bitop3:0x7e\n\t"
        "v_bitop3_b32 v86, v121, v123, %[t] bitop3:0x7e\n\t"
        "v_bitop3_b32 v87, v125, v127, %[t] bitop3:0x7e\n\t"
        "v_or_b32 v86, v86, v87\n\t"
        "v_cmp_eq_u32 s[74:75], 0, v78\n\t"
        "v_cmp_eq_u32 s[76:77], 0, v79\n\t"
        "v_cmp_eq_u32 s[78:79], 0, v80\n\t"
        "v_cmp_eq_u32 s[80:81], 0, v81\n\t"
        "v_cmp_eq_u32 s[82:83], 0, v82\n\t"
        "v_cmp_eq_u32 s[84:85], 0, v83\n\t"
        "v_cmp_eq_u32 s[86:87], 0, v84\n\t"
        "v_cmp_eq_u32 s[88:89], 0, v85\n\t"
        "v_cmp_eq_u32 s[90:91], 0, v86\n\t"
        "v_cndmask_b32_e64 %[o0], %[o0], 2.0, s[74:75]\n\t"
        "v_cndmask_b32_e64 %[o1], %[o1], 2.0, s[76:77]\n\t"
        "v_cndmask_b32_e64 %[o2], %[o2], 2.0, s[78:79]\n\t"
        "v_cndmask_b32_e64 %[o3], %[o3], 2.0, s[80:81]\n\t"
        "v_cndmask_b32_e64 %[o4], %[o4], 2.0, s[82:83]\n\t"
        "v_cndmask_b32_e64 %[o5], %[o5], 2.0, s[84:85]\n\t"
        "v_cndmask_b32_e64 %[o6], %[o6], 2.0, s[86:87]\n\t"
        "v_cndmask_b32_e64 %[o7], %[o7], 2.0, s[88:89]\n\t"
        "v_cndmask_b32_e64 %[o8], %[o8], 2.0, s[90:91]\n\t"
        "v_min3_u32 v78, %[o0], %[o1], %[o2]\n\t" "v_min3_u32 v79, %[o3], %[o4], %[o5]\n\t" "v_min3_u32 v80, %[o6], %[o7], %[o8]\n\t" "v_min3_u32 v78, v78, v79, v80\n\t"
        "v_cmp_eq_u32 vcc, 2.0, v78\n\t"
        "v_add_f32 v79, v120, v124\n\t" "v_add_f32 v80, v122, v126\n\t"
        "v_cndmask_b32_e64 %[a0], %[a0], v88, s[74:75]\n\t" "v_cndmask_b32_e64 %[b0], %[b0], v90, s[74:75]\n\t"
        "v_cndmask_b32_e64 %[a1], %[a1], v92, s[76:77]\n\t" "v_cndmask_b32_e64 %[b1], %[b1], v94, s[76:77]\n\t"
        "v_cndmask_b32_e64 %[a2], %[a2], v96, s[78:79]\n\t" "v_cndmask_b32_e64 %[b2], %[b2], v98, s[78:79]\n\t"
        "v_cndmask_b32_e64 %[a3], %[a3], v100, s[80:81]\n\t" "v_cndmask_b32_e64 %[b3], %[b3], v102, s[80:81]\n\t"
        "v_cndmask_b32_e64 %[a4], %[a4], v104, s[82:83]\n\t" "v_cndmask_b32_e64 %[b4], %[b4], v106, s[82:83]\n\t"
        "v_cndmask_b32_e64 %[a5], %[a5], v108, s[84:85]\n\t" "v_cndmask_b32_e64 %[b5], %[b5], v110, s[84:85]\n\t"
        "v_cndmask_b32_e64 %[a6], %[a6], v112, s[86:87]\n\t" "v_cndmask_b32_e64 %[b6], %[b6], v114, s[86:87]\n\t"
        "v_cndmask_b32_e64 %[a7], %[a7], v116, s[88:89]\n\t" "v_cndmask_b32_e64 %[b7], %[b7], v118, s[88:89]\n\t"
        "v_cndmask_b32_e64 %[f0], %[f0], v79, s[90:91]\n\t" "v_cndmask_b32_e64 %[f1], %[f1], v80, s[90:91]\n\t"
        "s_cmp_eq_u64 vcc, exec\n\t" "s_cbranch_scc1 2f\n\t"
        "buffer_load_dwordx4 v[88:91], %[o0], %[d], 0 offen sc1\n\t"
        "buffer_load_dwordx4 v[92:95], %[o1], %[d], 0 offen sc1\n\t"
        "buffer_load_dwordx4 v[96:99], %[o2], %[d], 0 offen sc1\n\t"
        "buffer_load_dwordx4 v[100:103], %[o3], %[d], 0 offen sc1\n\t"
        "buffer_load_dwordx4 v[104:107], %[o4], %[d], 0 offen sc1\n\t"
        "buffer_load_dwordx4 v[108:111], %[o5], %[d], 0 offen sc1\n\t"
        "buffer_load_dwordx4 v[112:115], %[o6], %[d], 0 offen sc1\n\t"
        "buffer_load_dwordx4 v[116:119], %[o7], %[d], 0 offen sc1\n\t"
        "buffer_load_dwordx4 v[120:123], %[o8], %[d], 0 offen sc1\n\t"
        "buffer_load_dwordx4 v[124:127], %[o8], %[d], s73 offen sc1\n\t"
        "s_waitcnt vmcnt(10)\n\t"
        "v_bitop3_b32 v78, v129, v131, %[t] bitop3:0x7e\n\t"
        "v_bitop3_b32 v79, v133, v135, %[t] bitop3:0x7e\n\t"
        "v_bitop3_b32 v80, v137, v139, %[t] bitop3:0x7e\n\t"
        "v_bitop3_b32 v81, v141, v143, %[t] bitop3:0x7e\n\t"
        "v_bitop3_b32 v82, v145, v147, %[t] bitop3:0x7e\n\t"
        "v_bitop3_b32 v83, v149, v151, %[t] bitop3:0x7e\n\t"
        "v_bitop3_b32 v84, v153, v155, %[t] bitop3:0x7e\n\t"
        "v_bitop3_b32 v85, v157, v159, %[t] bitop3:0x7e\n\t"
        "v_bitop3_b32 v86, v161, v163, %[t] bitop3:0x7e\n\t"
        "v_bitop3_b32 v87, v165, v167, %[t] bitop3:0x7e\n\t"
        "v_or_b32 v86, v86, v87\n\t"
        "v_cmp_eq_u32 s[74:75], 0, v78\n\t"
        "v_cmp_eq_u32 s[76:77], 0, v79\n\t"
        "v_cmp_eq_u32 s[78:79], 0, v80\n\t"
        "v_cmp_eq_u32 s[80:81], 0, v81\n\t"
        "v_cmp_eq_u32 s[82:83], 0, v82\n\t"
        "v_cmp_eq_u32 s[84:85], 0, v83\n\t"
        "v_cmp_eq_u32 s[86:87], 0, v84\n\t"
        "v_cmp_eq_u32 s[88:89], 0, v85\n\t"
        "v_cmp_eq_u32 s[90:91], 0, v86\n\t"
        "v_cndmask_b32_e64 %[o0], %[o0], 2.0, s[74:75]\n\t"
        "v_cndmask_b32_e64 %[o1], %[o1], 2.0, s[76:77]\n\t"
        "v_cndmask_b32_e64 %[o2], %[o2], 2.0, s[78:79]\n\t"
        "v_cndmask_b32_e64 %[o3], %[o3], 2.0, s[80:81]\n\t"
        "v_cndmask_b32_e64 %[o4], %[o4], 2.0, s[82:83]\n\t"
        "v_cndmask_b32_e64 %[o5], %[o5], 2.0, s[84:85]\n\t"
        "v_cndmask_b32_e64 %[o6], %[o6], 2.0, s[86:87]\n\t"
        "v_cndmask_b32_e64 %[o7], %[o7], 2.0, s[88:89]\n\t"
        "v_cndmask_b32_e64 %[o8], %[o8], 2.0, s[90:91]\n\t"
        "v_min3_u32 v78, %[o0], %[o1], %[o2]\n\t" "v_min3_u32 v79, %[o3], %[o4], %[o5]\n\t" "v_min3_u32 v80, %[o6], %[o7], %[o8]\n\t" "v_min3_u32 v78, v78, v79, v80\n\t"
        "v_cmp_eq_u32 vcc, 2.0, v78\n\t"
        "v_add_f32 v79, v160, v164\n\t" "v_add_f32 v80, v162, v166\n\t"
        "v_cndmask_b32_e64 %[a0], %[a0], v128, s[74:75]\n\t" "v_cndmask_b32_e64 %[b0], %[b0], v130, s[74:75]\n\t"
        "v_cndmask_b32_e64 %[a1], %[a1], v132, s[76:77]\n\t" "v_cndmask_b32_e64 %[b1], %[b1], v134, s[76:77]\n\t"
        "v_cndmask_b32_e64 %[a2], %[a2], v136, s[78:79]\n\t" "v_cndmask_b32_e64 %[b2], %[b2], v138, s[78:79]\n\t"
        "v_cndmask_b32_e64 %[a3], %[a3], v140, s[80:81]\n\t" "v_cndmask_b32_e64 %[b3], %[b3], v142, s[80:81]\n\t"
        "v_cndmask_b32_e64 %[a4], %[a4], v144, s[82:83]\n\t" "v_cndmask_b32_e64 %[b4], %[b4], v146, s[82:83]\n\t"
        "v_cndmask_b32_e64 %[a5], %[a5], v148, s[84:85]\n\t" "v_cndmask_b32_e64 %[b5], %[b5], v150, s[84:85]\n\t"
        "v_cndmask_b32_e64 %[a6], %[a6], v152, s[86:87]\n\t" "v_cndmask_b32_e64 %[b6], %[b6], v154, s[86:87]\n\t"
        "v_cndmask_b32_e64 %[a7], %[a7], v156, s[88:89]\n\t" "v_cndmask_b32_e64 %[b7], %[b7], v158, s[88:89]\n\t"
        "v_cndmask_b32_e64 %[f0], %[f0], v79, s[90:91]\n\t" "v_cndmask_b32_e64 %[f1], %[f1], v80, s[90:91]\n\t"
        "s_cmp_eq_u64 vcc, exec\n\t" "s_cbranch_scc1 2f\n\t"
        "s_sub_u32 s72, s72, 1\n\t" "s_cbranch_scc0 1b\n\t"
        "2:\n\t" "s_nop 1 ; POLL_END"
        : [o0] "+v"(o[0]), [o1] "+v"(o[1]), [o2] "+v"(o[2]), [o3] "+v"(o[3]), [o4] "+v"(o[4]), [o5] "+v"(o[5]), [o6] "+v"(o[6]), [o7] "+v"(o[7]), [o8] "+v"(o[8]),
          [a0] "+v"(pa[0]), [a1] "+v"(pa[1]), [a2] "+v"(pa[2]), [a3] "+v"(pa[3]), [a4] "+v"(pa[4]), [a5] "+v"(pa[5]), [a6] "+v"(pa[6]), [a7] "+v"(pa[7]),
          [b0] "+v"(pb[0]), [b1] "+v"(pb[1]), [b2] "+v"(pb[2]), [b3] "+v"(pb[3]), [b4] "+v"(pb[4]), [b5] "+v"(pb[5]), [b6] "+v"(pb[6]), [b7] "+v"(pb[7]), [f0] "+v"(f0), [f1] "+v"(f1)
        : [d] "s"(dws), [t] "v"(tag)
        : "memory", "vcc", "scc", "s72", "s73", "s74", "s75", "s76", "s77", "s78", "s79", "s80", "s81", "s82", "s83", "s84", "s85", "s86", "s87", "s88", "s89", "s90", "s91", "v78", "v79", "v80", "v81", "v82", "v83", "v84", "v85", "v86", "v87", "v88", "v89", "v90", "v91", "v92", "v93", "v94", "v95", "v96", "v97", "v98", "v99", "v100", "v101", "v102", "v103", "v104", "v105", "v106", "v107", "v108", "v109", "v110", "v111", "v112", "v113", "v114", "v115", "v116", "v117", "v118", "v119", "v120", "v121", "v122", "v123", "v124", "v125", "v126", "v127", "v128", "v129", "v130", "v131", "v132", "v133", "v134", "v135", "v136", "v137", "v138", "v139", "v140", "v141", "v142", "v143", "v144", "v145", "v146", "v147", "v148", "v149", "v150", "v151", "v152", "v153", "v154", "v155", "v156", "v157", "v158", "v159", "v160", "v161", "v162", "v163", "v164", "v165", "v166", "v167");
    const float s0 = ((pa[0] + pa[1]) + (pa[2] + pa[3])) + ((pa[4] + pa[5]) + (pa[6] + pa[7]));
    const float s1 = ((pb[0] + pb[1]) + (pb[2] + pb[3])) + ((pb[4] + pb[5]) + (pb[6] + pb[7]));
    const float f = rbf(pair_sum(f0, f1)), gate = rbf(f * rcp(1.f + ex2(-LOG2E * f)));
    const float res = xres + rbf(rbf(pair_sum(s0, s1)) * gate);
    __builtin_amdgcn_raw_buffer_store_b16((u16)cvtpk(res, res), rout, oo, 0, 0);
    __builtin_amdgcn_raw_buffer_store_b32(tag, rws, oe, 0, SC1);
    asm volatile("s_waitcnt vmcnt(0) ; POLL_DRAIN" ::: "memory");
    EXIT_WAVE
  }

  LANE_IDS
  const int w = wave;
  __builtin_amdgcn_s_setprio(2);
  const int rowl = 32 * (n >> 3) + 8 * g, chk = (n & 7) * 16;
  const int afo = ((n >> 2) & 1) * 64 + g * 16;
  f4 acc[8];
#pragma unroll
  for (int j = 0; j < 8; ++j) acc[j] = f4{0.f, 0.f, 0.f, 0.f};
  if (__builtin_expect(isS, 1)) {
    const u16* base = c < 64 ? wq + c * 64 : c < 136 ? wg + (c - 64) * 64 : wu + (c - 136) * 64;
    const int stride = c < 64 ? NQ * 2 : FF * 2;
    const __amdgpu_buffer_rsrc_t rw = mkrsrc(base, (DM - 1) * stride + 128);
    const int voff = rowl * stride + chk;
    const unsigned tbase = tstart + (unsigned)(((2 * w - AHEAD) * TAU) >> 8);
    v4u R[24];
#pragma unroll
    for (int i = 0; i < 24; ++i) {
      if ((i & 1) == 0 && 3 * i > AHEAD) PACE(3 * i);
      R[i] = ld_nt(rw, voff, (192 * w + 64 * (i >> 3) + (i & 7)) * stride);
    }
    __builtin_amdgcn_sched_barrier(0);
    lds_wait<1>(FL + F_XN, 1);
#pragma unroll
    for (int gi = 0; gi < 3; ++gi)
      gemv_group(R + 8 * gi, *(const LAS v4u*)(L + L_XN + (n & 3) * XN_STR + (3 * w + gi) * 128 + afo), acc);
    *(LAS f4*)(L + L_S1R + w * 1024 + lane * 16) = gemv_finish(acc, lane);
    const unsigned tag = FL[F_TAG];
    if (lds_arrive(F_S1) == 2) {
      const f4 o = (*(const LAS f4*)(L + L_S1R + lane * 16) + *(const LAS f4*)(L + L_S1R + 1024 + lane * 16)) + *(const LAS f4*)(L + L_S1R + 2048 + lane * 16);
      st_sc1(v4u{cvtpk(o[0], o[1]), tag, cvtpk(o[2], o[3]), tag}, rws,
             WS_S1 + (2 * (n >> 3) + (g & 1)) * S1_ROW + (64 * c + 8 * (n & 7) + 4 * (g >> 1)) * 4);
    }
    if (c >= N_WO) EXIT_WAVE
    lds_wait<1>(FL + F_D, WOCNT);
    const __amdgpu_buffer_rsrc_t ro = mkrsrc(wo + (size_t)ksl * 64 * DM + (3 * cs + w) * 64, 63 * DM * 2 + 128);
    v4u W[8], T[8];
#pragma unroll
    for (int i = 0; i < 8; ++i) W[i] = ld_nt(ro, rowl * (DM * 2) + chk + i * (DM * 2));
#pragma unroll
    for (int q = 0; q < 4; ++q)
#pragma unroll
      for (int p = 0; p < 4; ++p) {
        T[2 * q][p] = __builtin_amdgcn_perm(W[2 * p + 1][q], W[2 * p][q], 0x05040100u);
        T[2 * q + 1][p] = __builtin_amdgcn_perm(W[2 * p + 1][q], W[2 * p][q], 0x07060302u);
      }
    int rec = WS_H2 + (cs * 64 + ksl) * 6144 + w * 2048 + lane * 16;
    asm volatile("" : "+v"(T[0]), "+v"(T[1]), "+v"(T[2]), "+v"(T[3]), "+v"(T[4]), "+v"(T[5]), "+v"(T[6]), "+v"(T[7]), "+v"(rec));
    v4u alo, ahi;
    {
      const int af = L_AF + lane * 16;
      unsigned f_, s_;
      asm volatile("s_mov_b32 s72, %7\n1:\n\tds_read_b32 %2, %4\n\tds_read_b128 %0, %5\n\tds_read_b128 %1, %6\n\ts_waitcnt lgkmcnt(2)\n\t"
                   "v_readfirstlane_b32 %3, %2\n\ts_cmp_lg_u32 %3, 0\n\ts_cbranch_scc1 2f\n\ts_sub_u32 s72, s72, 1\n\ts_cbranch_scc0 1b\n2:\n\ts_waitcnt lgkmcnt(0)"
                   : "=&v"(alo), "=&v"(ahi), "=&v"(f_), "=&s"(s_)
                   : "v"(L_FLAG + 4 * F_A), "v"(n < 8 ? af : L_ZERO), "v"(n < 8 ? L_ZERO : af), "i"(LDS_SPIN) : "memory", "scc", "s72");
    }
#pragma unroll
    for (int j = 0; j < 4; ++j) acc[j] = mfma(alo, T[j], f4{0.f, 0.f, 0.f, 0.f});
#pragma unroll
    for (int j = 0; j < 4; ++j) acc[j] = mfma(ahi, T[j + 4], acc[j]);
    const f4 G = gemv_fold(acc, lane);
    st_sc1(v4u{__float_as_uint(G[0]), tag, __float_as_uint(G[1]), tag}, rws, rec);
    st_sc1(v4u{__float_as_uint(G[2]), tag, __float_as_uint(G[3]), tag}, rws, rec + 1024);
    lds_post(F_H2);
  } else {
    const int ds = (c - N_S1) % 3, rs = (c - N_S1) / 3;
    const __amdgpu_buffer_rsrc_t rd = mkrsrc(wd + (size_t)rs * 288 * DM + (3 * ds + w) * 64, 287 * DM * 2 + 128);
    const int voff = rowl * (DM * 2) + chk, voff4 = n >= 8 ? OOB : voff;
    v4u R[24];
#pragma unroll
    for (int i = 0; i < 8; ++i) R[i] = ld_nt(rd, voff, i * (DM * 2));
    __builtin_amdgcn_sched_barrier(0);
    lds_wait<1>(FL + F_ISS, 8);
#pragma unroll
    for (int i = 8; i < 24; ++i) R[i] = ld_nt(rd, voff, (64 * (i >> 3) + (i & 7)) * (DM * 2));
    __builtin_amdgcn_sched_barrier(0);
    lds_wait<1>(FL + F_H, 1);
    const unsigned tag = FL[F_TAG];
    const int ao = L_H + (n & 3) * H_STR + afo;
    gemv_group(R, *(const LAS v4u*)(L + ao), acc);
    __builtin_amdgcn_sched_barrier(0);
#pragma unroll
    for (int i = 0; i < 8; ++i) R[i] = ld_nt(rd, voff, (192 + i) * (DM * 2));
    __builtin_amdgcn_sched_barrier(0);
    gemv_group(R + 8, *(const LAS v4u*)(L + ao + 128), acc);
    __builtin_amdgcn_sched_barrier(0);
#pragma unroll
    for (int i = 0; i < 8; ++i) R[8 + i] = ld_nt(rd, voff4, (256 + i) * (DM * 2));
    __builtin_amdgcn_sched_barrier(0);
    gemv_group(R + 16, *(const LAS v4u*)(L + ao + 256), acc);
    gemv_group(R, *(const LAS v4u*)(L + ao + 384), acc);
    gemv_group(R + 8, *(const LAS v4u*)(L + ao + 512), acc);
    const f4 G = gemv_finish(acc, lane);
    const int rec = WS_F2 + (ds * 16 + rs) * 6144 + w * 2048 + lane * 16;
    st_sc1(v4u{__float_as_uint(G[0]), tag, __float_as_uint(G[1]), tag}, rws, rec);
    st_sc1(v4u{__float_as_uint(G[2]), tag, __float_as_uint(G[3]), tag}, rws, rec + 1024);
  }
}

extern "C" void kernel_launch(void* const* d_in, const int* in_sizes, int n_in, void* d_out, int out_size, void* d_ws,
                              size_t ws_size, hipStream_t stream) {
  fused_decode<<<NCU, NTHR, 0, stream>>>((const u16*)d_in[0], (const u16*)d_in[1], (const u16*)d_in[2], (const u16*)d_in[3],
                                         (const u16*)d_in[4], (const u16*)d_in[6], (const u16*)d_in[7], (const u16*)d_in[8],
                                         (u16*)d_out, (unsigned char*)d_ws);
}
```

```cpp
#include <hip/hip_runtime.h>
#include <stdint.h>

typedef unsigned short u16;
typedef unsigned v4u __attribute__((ext_vector_type(4)));
typedef unsigned v2u __attribute__((ext_vector_type(2)));
typedef float f4 __attribute__((ext_vector_type(4)));
typedef short s8v __attribute__((ext_vector_type(8)));
typedef __bf16 bf2v __attribute__((ext_vector_type(2)));
#define LAS __attribute__((address_space(3)))
#define GAS __attribute__((address_space(1)))
#define STR2(x) #x
#define STR(x) STR2(x)

constexpr int DM = 576, SK = 8192, WIN = 2048, FF = 4608, NQ = 4096;
constexpr int NCU = 256, NTHR = 768;
constexpr int N_S1 = 208, N_WO = 192, N_FIN = 144;
constexpr int OOB = 0x40000000;
constexpr int NT = 2, SC1 = 16;
constexpr float LOG2E = 1.4426950408889634f, QK_C = 0.08838834764831845f * LOG2E;

#define TAU 1008
#define AHEAD 28
#define TAUD 850
#define AHD 12
#define DSTEP 10u
#define T_A 1355
#define T_B 1475
#define D_AB 26
#define VSPLIT 10
#define WOCNT 4
#define STAG 6
#define CODEPF 23552

constexpr int S1_ROW = (NQ + 2 * FF) * 4;
constexpr int WS_EPOCH = 0, WS_T0 = 1024, WS_S1 = 4096, WS_H1 = WS_S1 + 4 * S1_ROW, WS_H2 = WS_H1 + NCU * 1024,
              WS_F2 = WS_H2 + 3 * 64 * 6144, WS_END = WS_F2 + 3 * 16 * 6144;
constexpr int XN_STR = 1168, H_STR = 656;
constexpr int L_XN = 0, L_H = L_XN + 4 * XN_STR, L_Q = L_H + 4 * H_STR, L_S1R = L_Q + 512, L_AM = L_S1R + 3 * 1024,
              L_AL = L_AM + 64, L_AD = L_AL + 64, L_AF = L_AD + 8 * 1024, L_XS = L_AF + 1024, L_DUM = L_XS + 5376,
              L_ZERO = L_DUM + 1024, L_FLAG = L_ZERO + 256, L_END = L_FLAG + 64;
enum { F_XN = 0, F_TAG, F_Q, F_H, F_S1, F_ISS, F_D, F_PUB, F_L, F_A, F_H2 };
static_assert(L_H % 16 == 0 && L_Q % 16 == 0 && L_S1R % 16 == 0 && L_AM % 16 == 0 && L_AD % 16 == 0 && L_XS % 16 == 0 &&
              L_DUM % 16 == 0 && L_ZERO % 16 == 0, "align");
#define GLB_N 65536
constexpr unsigned LDS_SPIN = 1u << 22, GLB_SPIN = GLB_N;

#define PACE(kb) asm volatile("1:\n\ts_memrealtime s[92:93]\n\ts_waitcnt lgkmcnt(0)\n\ts_sub_u32 s92, s92, %0\n\ts_cmp_lt_i32 s92, %1\n\t" \
    "s_cbranch_scc0 2f\n\ts_sleep 1\n\ts_branch 1b\n2:" :: "s"(tbase), "i"((int)(((kb) * TAU) >> 8)) : "scc", "memory", "s92", "s93")
#define PACE_A(kb) asm volatile("1:\n\ts_memrealtime s[92:93]\n\ts_waitcnt lgkmcnt(0)\n\ts_sub_u32 s92, s92, %0\n\ts_cmp_lt_i32 s92, %1\n\t" \
    "s_cbranch_scc0 2f\n\ts_sleep 1\n\ts_branch 1b\n2:\n\ts_add_u32 %0, %0, %2" : "+s"(tbase) : "i"((int)(((kb) * TAU) >> 8)), "s"(dstep) : "scc", "memory", "s92", "s93")
#define PLOAD(d, voff, soff) asm volatile("buffer_load_dwordx4 %0, %1, %2, %3 offen sc1" : "=v"(d) : "v"(voff), "s"(dws), "s"(soff) : "memory")
#define EXIT_WAVE { __builtin_amdgcn_endpgm(); __builtin_unreachable(); }

__device__ __forceinline__ __amdgpu_buffer_rsrc_t mkrsrc(const void* p, unsigned bytes) {
  return __builtin_amdgcn_make_buffer_rsrc((void*)p, 0, bytes, 0x00020000);
}
__device__ __forceinline__ v4u ld_nt(__amdgpu_buffer_rsrc_t r, int voff, int soff = 0) {
  return __builtin_amdgcn_raw_buffer_load_b128(r, voff, soff, NT);
}
__device__ __forceinline__ void st_sc1(v4u v, __amdgpu_buffer_rsrc_t r, int voff) {
  __builtin_amdgcn_raw_buffer_store_b128(v, r, voff, 0, SC1);
}
__device__ __forceinline__ f4 mfma(v4u a, v4u b, f4 c) {
  return __builtin_amdgcn_mfma_f32_16x16x32_bf16(__builtin_bit_cast(s8v, a), __builtin_bit_cast(s8v, b), c, 0, 0, 0);
}
__device__ __forceinline__ unsigned cvtpk(float lo, float hi) {
  bf2v r = {(__bf16)lo, (__bf16)hi};
  return __builtin_bit_cast(unsigned, r);
}
__device__ __forceinline__ float rbf(float f) { return (float)(__bf16)f; }
__device__ __forceinline__ float rcp(float f) { return __builtin_amdgcn_rcpf(f); }
__device__ __forceinline__ float ex2(float f) { return __builtin_amdgcn_exp2f(f); }
__device__ __forceinline__ float blo(unsigned u) { return __uint_as_float(u << 16); }
__device__ __forceinline__ float bhi(unsigned u) { return __uint_as_float(u & 0xffff0000u); }
__device__ __forceinline__ unsigned mktag(unsigned e) { return ((e + 1u) & 0xffffu) ? e + 1u : e + 2u; }
template <int CTRL> __device__ __forceinline__ float dpp(float x) {
  return __builtin_bit_cast(float, __builtin_amdgcn_mov_dpp(__builtin_bit_cast(int, x), CTRL, 0xf, 0xf, true));
}
constexpr int ROR1 = 0x121, ROR2 = 0x122, ROR4 = 0x124, ROR8 = 0x128, XOR1 = 0xB1;
__device__ __forceinline__ float row_sum(float x) { x += dpp<ROR8>(x); x += dpp<ROR4>(x); x += dpp<ROR2>(x); x += dpp<ROR1>(x); return x; }
__device__ __forceinline__ float row_max(float x) {
  x = fmaxf(x, dpp<ROR8>(x)); x = fmaxf(x, dpp<ROR4>(x)); x = fmaxf(x, dpp<ROR2>(x)); x = fmaxf(x, dpp<ROR1>(x)); return x; }
__device__ __forceinline__ void swap16(float& a, float& b) {
  auto s = __builtin_amdgcn_permlane16_swap(__float_as_uint(a), __float_as_uint(b), false, false);
  a = __uint_as_float(s[0]); b = __uint_as_float(s[1]);
}
__device__ __forceinline__ void swap32(float& a, float& b) {
  auto s = __builtin_amdgcn_permlane32_swap(__float_as_uint(a), __float_as_uint(b), false, false);
  a = __uint_as_float(s[0]); b = __uint_as_float(s[1]);
}
__device__ __forceinline__ float xrow_sum(float x) { float a = x, b = x; swap16(a, b); x = a + b; a = x; b = x; swap32(a, b); return a + b; }
__device__ __forceinline__ float xrow_max(float x) { float a = x, b = x; swap16(a, b); x = fmaxf(a, b); a = x; b = x; swap32(a, b); return fmaxf(a, b); }
__device__ __forceinline__ float pair_sum(float a, float b) {
  a += dpp<ROR8>(a); b += dpp<ROR8>(b);
  swap16(a, b);
  float t = a + b, u = t;
  swap32(t, u);
  return t + u;
}

template <int SLEEP> __device__ __forceinline__ void lds_wait(volatile LAS unsigned* f, unsigned want) {
#pragma nounroll
  for (unsigned s = 0; s < LDS_SPIN; ++s) {
    if ((unsigned)__builtin_amdgcn_readfirstlane(*f) >= want) break;
    if (SLEEP) __builtin_amdgcn_s_sleep(SLEEP);
  }
  asm volatile("" ::: "memory");
}
__device__ __forceinline__ void wait_until(int ticks, unsigned t0) {
  asm volatile("1:\n\ts_memrealtime s[92:93]\n\ts_waitcnt lgkmcnt(0)\n\ts_sub_u32 s92, s92, %0\n\ts_cmp_lt_i32 s92, %1\n\ts_cbranch_scc1 1b"
               :: "s"(t0), "s"(ticks) : "scc", "memory", "s92", "s93");
}
__device__ __forceinline__ void lds_post(int flag) {
  asm volatile("s_mov_b64 exec, 1\n\tds_add_u32 %0, %1\n\ts_mov_b64 exec, -1" :: "v"(L_FLAG + 4 * flag), "v"(1u) : "memory");
}
__device__ __forceinline__ unsigned lds_arrive(int flag) {
  unsigned o, r;
  asm volatile("s_mov_b64 exec, 1\n\tds_add_rtn_u32 %0, %2, %3\n\ts_mov_b64 exec, -1\n\ts_waitcnt lgkmcnt(0)\n\tv_readfirstlane_b32 %1, %0"
               : "=&v"(o), "=s"(r) : "v"(L_FLAG + 4 * flag), "v"(1u) : "memory");
  return r;
}

__device__ __forceinline__ void gemv_group(const v4u* r, v4u a, f4* acc) {
#pragma unroll
  for (int w = 0; w < 4; ++w) {
    v4u lo, hi;
#pragma unroll
    for (int p = 0; p < 4; ++p) {
      lo[p] = __builtin_amdgcn_perm(r[2 * p + 1][w], r[2 * p][w], 0x05040100u);
      hi[p] = __builtin_amdgcn_perm(r[2 * p + 1][w], r[2 * p][w], 0x07060302u);
    }
    acc[2 * w] = mfma(a, lo, acc[2 * w]);
    acc[2 * w + 1] = mfma(a, hi, acc[2 * w + 1]);
  }
}
__device__ __forceinline__ f4 gemv_fold(const f4* e, int lane) {
  f4 G;
#pragma unroll
  for (int j = 0; j < 4; ++j) {
    float e0 = e[j][0], e1 = e[j][1], e2 = e[j][2], e3 = e[j][3];
    swap16(e0, e1); swap16(e2, e3);
    const float t0 = e0 + dpp<ROR8>(e1), t1 = dpp<ROR8>(e2) + e3;
    G[j] = (lane & 8) ? t1 : t0;
  }
  return G;
}
__device__ __forceinline__ f4 gemv_finish(const f4* acc, int lane) {
  f4 e[4];
#pragma unroll
  for (int j = 0; j < 4; ++j) e[j] = lane < 32 ? acc[j] : acc[j + 4];
  return gemv_fold(e, lane);
}

__global__ void __launch_bounds__(NTHR, 3)
fused_decode(const u16* __restrict__ x, const u16* __restrict__ kc, const u16* __restrict__ vc, const u16* __restrict__ wq,
             const u16* __restrict__ wo, const u16* __restrict__ wg, const u16* __restrict__ wu, const u16* __restrict__ wd,
             u16* __restrict__ out, unsigned char* ws) {
  __shared__ __attribute__((aligned(16))) unsigned char lds_raw[L_END];
  LAS unsigned char* L = (LAS unsigned char*)lds_raw;
  volatile LAS unsigned* FL = (volatile LAS unsigned*)(L + L_FLAG);
  const int c = blockIdx.x;
  const bool isS = c < N_S1;
  const int pair = (c & 7) * 8 + (c >> 5), sp = (c >> 3) & 3;
  int ksl = (c & 7) * 8 + (c >> 3) / 3, cs = (c >> 3) % 3;
  asm volatile("" : "+s"(ksl), "+s"(cs));
  *(volatile LAS unsigned*)(L + L_ZERO + min(threadIdx.x, 79u) * 4) = 0u;
  unsigned long long tstart64;
  asm volatile("s_memrealtime %0\n\ts_waitcnt lgkmcnt(0)\n\ts_barrier" : "=&s"(tstart64)
               : "s"(x), "s"(kc), "s"(vc), "s"(wq), "s"(wo), "s"(wg), "s"(wu), "s"(wd), "s"(out), "s"(ws) : "memory");
  const unsigned tstart = (unsigned)tstart64;
  const int wave = __builtin_amdgcn_readfirstlane(threadIdx.x >> 6);
#define LANE_IDS int lane_ = threadIdx.x & 63; asm volatile("" : "+v"(lane_)); const int lane = lane_, g = lane >> 4, n = lane & 15;
  const __amdgpu_buffer_rsrc_t rws = mkrsrc(ws, WS_END);
  if (wave == 3) {
    const v4u dx = {(unsigned)(uintptr_t)x, (unsigned)((uintptr_t)x >> 32), 4u * DM * 2u, 0x00020000u};
    const v4u de = {(unsigned)(uintptr_t)ws, (unsigned)((uintptr_t)ws >> 32), 4u, 0x00020000u};
    const int xo = (threadIdx.x & 63) * 16, xo4 = xo < 512 ? xo + 4096 : OOB, z = 0;
    unsigned keep;
    asm volatile("s_mov_b32 %0, m0\n\ts_mov_b32 m0, %4\n\ts_nop 0\n\t"
                 "buffer_load_dwordx4 %1, %2, 0 offen lds\n\tbuffer_load_dwordx4 %1, %2, 0 offen offset:1024 lds\n\t"
                 "buffer_load_dwordx4 %1, %2, 0 offen offset:2048 lds\n\tbuffer_load_dwordx4 %1, %2, 0 offen offset:3072 lds\n\t"
                 "s_mov_b32 m0, %5\n\ts_nop 0\n\tbuffer_load_dwordx4 %8, %2, 0 offen lds\n\t"
                 "s_mov_b32 m0, %6\n\ts_nop 0\n\tbuffer_load_dword %7, %3, 0 offen sc1 lds\n\ts_mov_b32 m0, %0"
                 : "=&s"(keep) : "v"(xo), "s"(dx), "s"(de), "i"(L_XS), "i"(L_XS + 4096), "i"(L_XS + 5120), "v"(z), "v"(xo4) : "memory");
  }
  if (wave == 11) {
    const int po = (c >> 3) * 1024 + (threadIdx.x & 63) * 16;
    unsigned keep;
    asm volatile("s_mov_b32 %0, m0\n\ts_getpc_b64 s[92:93]\n\ts_and_b32 s92, s92, 0xffffff00\n\ts_mov_b32 s94, %2\n\ts_mov_b32 s95, 0x20000\n\t"
                 "s_mov_b32 m0, %3\n\ts_nop 0\n\tbuffer_load_dwordx4 %1, s[92:95], 0 offen lds\n\ts_mov_b32 m0, %0"
                 : "=&s"(keep) : "v"(po), "i"(CODEPF), "i"(L_DUM) : "memory", "scc", "s92", "s93", "s94", "s95");
  }

  if (__builtin_expect(wave >= 4, 0)) {
    LANE_IDS
    const int w = wave - 4;
    const size_t kvo = ((size_t)pair * SK + (SK - WIN) + sp * 512 + ((w + pair) & 7) * 64) * 128;
    const __amdgpu_buffer_rsrc_t rk = mkrsrc(kc + kvo, 16384), rv = mkrsrc(vc + kvo, 16384);
    const int kvo0 = (n >> 2) * 4096 + (n >> 1) * 256 + (n & 1) * 64 + g * 16, vvo0 = g * 4096 + n * 16;
    unsigned tbase = tstart + (unsigned)(((((isS ? 72 : 24) - AHEAD) + 2 * w) * TAU) >> 8);
    const unsigned dstep = isS ? 0u : DSTEP;
    v4u K[16], V[16];
#pragma unroll
    for (int i = 0; i < 16; ++i) {
      if ((i & 1) == 0) PACE_A(8 * i);
      K[i] = ld_nt(rk, (kvo0 ^ ((i >> 2) * 4096)) + ((i >> 1) & 1) * 2048 + (i & 1) * 128);
    }
#define V_LOAD(i) { if (((i) & 1) == 0) PACE_A(128 + 8 * (i)); V[i] = ld_nt(rv, (vvo0 ^ (((i) >> 2) * 4096)) + (((i) & 1) * 8 + ((i) >> 1)) * 256); }
#pragma unroll
    for (int i = 0; i < VSPLIT; ++i) V_LOAD(i)
    __builtin_amdgcn_sched_barrier(0);
    float m0, m1; unsigned pk;
    auto scores = [&]() __attribute__((always_inline)) {
      f4 E0 = f4{0.f, 0.f, 0.f, 0.f}, E1 = f4{0.f, 0.f, 0.f, 0.f};
      const int qa = L_Q + ((n >> 1) & 1) * 256 + (n & 1) * 64 + g * 16;
#pragma unroll
      for (int tp = 0; tp < 4; ++tp) {
        const int ad = (n >> 2) == tp ? qa : L_ZERO;
        const v4u q0 = *(const LAS v4u*)(L + ad), q1 = *(const LAS v4u*)(L + ad + 128);
        E0 = mfma(q0, K[4 * tp], E0); E1 = mfma(q0, K[4 * tp + 2], E1);
        E0 = mfma(q1, K[4 * tp + 1], E0); E1 = mfma(q1, K[4 * tp + 3], E1);
      }
      const float s0 = ((n & 1) ? E1[1] : E0[0]) + dpp<XOR1>((n & 1) ? E0[1] : E1[0]);
      const float s1 = ((n & 1) ? E1[3] : E0[2]) + dpp<XOR1>((n & 1) ? E0[3] : E1[2]);
      m0 = xrow_max(row_max(s0)); m1 = xrow_max(row_max(s1));
      const float p0 = ex2((s0 - m0) * QK_C), p1 = ex2((s1 - m1) * QK_C);
      const float l0 = xrow_sum(row_sum(p0)), l1 = xrow_sum(row_sum(p1));
      if (lane == 0) {
        *(LAS float*)(L + L_AM + w * 4) = m0; *(LAS float*)(L + L_AM + 32 + w * 4) = m1;
        *(LAS float*)(L + L_AL + w * 4) = l0; *(LAS float*)(L + L_AL + 32 + w * 4) = l1;
      }
      lds_post(F_L);
      pk = cvtpk(p0, p1);
    };
    const bool early = __builtin_amdgcn_readfirstlane(FL[F_Q]) != 0;
    asm volatile("" ::: "memory");
    if (early) scores();
    __builtin_amdgcn_sched_barrier(0);
#pragma unroll
    for (int i = VSPLIT; i < 16; ++i) V_LOAD(i)
    __builtin_amdgcn_sched_barrier(0);
    lds_post(F_ISS);
    if (!early) { lds_wait<1>(FL + F_Q, 1); scores(); }
    lds_wait<1>(FL + F_L, 8);
    float scale; unsigned t1, t3;
    {
      const int h = g >> 1;
      const f4 ma = *(const LAS f4*)(L + L_AM + h * 32), mb = *(const LAS f4*)(L + L_AM + h * 32 + 16);
      const f4 la = *(const LAS f4*)(L + L_AL + h * 32), lb = *(const LAS f4*)(L + L_AL + h * 32 + 16);
      const float Mh = fmaxf(fmaxf(fmaxf(ma[0], ma[1]), fmaxf(ma[2], ma[3])), fmaxf(fmaxf(mb[0], mb[1]), fmaxf(mb[2], mb[3])));
      float Lh = 0.f;
#pragma unroll
      for (int k = 0; k < 4; ++k) Lh = fmaf(la[k], ex2((ma[k] - Mh) * QK_C), Lh);
#pragma unroll
      for (int k = 0; k < 4; ++k) Lh = fmaf(lb[k], ex2((mb[k] - Mh) * QK_C), Lh);
      scale = ex2(((h ? m1 : m0) - Mh) * QK_C) * rcp(Lh);
      const unsigned th = FL[F_TAG] << 16, ml = __float_as_uint((g & 1) ? Lh : Mh);
      t1 = th | (ml & 0xffffu); t3 = th | (ml >> 16);
    }
    int oh = WS_H1 + (pair * 4 + sp) * 1024 + (g >> 1) * 512 + n * 32 + (g & 1) * 16, la_ = L_AD + lane * 16;
    const unsigned hs = (n & 8) ? 0x0302u : 0x0100u, one = (n & 1) ? ((hs << 16) | 0x0c0cu) : (0x0c0c0000u | hs);
    unsigned sel[4];
#pragma unroll
    for (int r = 0; r < 4; ++r) sel[r] = ((n & 7) >> 1) == r ? one : 0x0c0c0c0cu;
    asm volatile("" : "+v"(scale), "+v"(t1), "+v"(t3), "+v"(oh), "+v"(la_));
    __builtin_amdgcn_s_setprio(3);
    __builtin_amdgcn_sched_barrier(0);
    f4 O = f4{0.f, 0.f, 0.f, 0.f};
#define PV_STEP(i) { const unsigned pb = (unsigned)__builtin_amdgcn_mov_dpp((int)pk, 0x150 + (i), 0xf, 0xf, true); \
      v4u a; a[0] = __builtin_amdgcn_perm(pb, pb, sel[0]); a[1] = __builtin_amdgcn_perm(pb, pb, sel[1]); \
      a[2] = __builtin_amdgcn_perm(pb, pb, sel[2]); a[3] = __builtin_amdgcn_perm(pb, pb, sel[3]); O = mfma(a, V[i], O); }
    PV_STEP(0) PV_STEP(1) PV_STEP(2) PV_STEP(3) PV_STEP(4) PV_STEP(5) PV_STEP(6) PV_STEP(7)
    PV_STEP(8) PV_STEP(9) PV_STEP(10) PV_STEP(11) PV_STEP(12) PV_STEP(13) PV_STEP(14) PV_STEP(15)
    *(LAS f4*)(L + la_ + w * 1024) = O * scale;
    if (lds_arrive(F_D) == 7) {
      f4 t[8];
#pragma unroll
      for (int k = 0; k < 8; ++k) t[k] = *(const LAS f4*)(L + la_ + k * 1024);
      const f4 o = ((t[0] + t[1]) + (t[2] + t[3])) + ((t[4] + t[5]) + (t[6] + t[7]));
      st_sc1(v4u{cvtpk(o[0], o[1]), t1, cvtpk(o[2], o[3]), t3}, rws, oh);
      FL[F_PUB] = 1;
    }
    EXIT_WAVE
  }

  if (__builtin_expect(wave == 3, 0)) {
    __builtin_amdgcn_s_setprio(3);
    LANE_IDS
    const v4u dws = {(unsigned)(uintptr_t)ws, (unsigned)((uintptr_t)ws >> 32), (unsigned)WS_END, 0x00020000u};
    const int zero = 0;
    asm volatile("s_waitcnt vmcnt(0)" ::: "memory");
    const unsigned tag = mktag(*(const volatile LAS unsigned*)(L + L_XS + 5120));
    st_sc1(v4u{tstart, tag, tstart, tag}, rws, (c < 8 && lane < 16) ? WS_T0 + lane * 128 + c * 16 : OOB);
    if (isS) {
      float xf[40], s = 0.f;
#pragma unroll
      for (int i = 0; i < 5; ++i) {
        const v4u X = *(const volatile LAS v4u*)(L + ((i == 4 && n >= 8) ? L_ZERO : L_XS + g * (DM * 2) + (n + 16 * i) * 16));
#pragma unroll
        for (int k = 0; k < 4; ++k) { xf[i * 8 + 2 * k] = blo(X[k]); xf[i * 8 + 2 * k + 1] = bhi(X[k]); }
      }
#pragma unroll
      for (int i = 0; i < 40; ++i) s += xf[i];
      const float mean = row_sum(s) * (1.f / DM);
      float v = 0.f;
#pragma unroll
      for (int i = 0; i < 40; ++i) { const float d = (i >= 32 && n >= 8) ? 0.f : xf[i] - mean; v = fmaf(d, d, v); }
      const float var = row_sum(v) * (1.f / DM);
      const float mb = rbf(mean), rden = rcp(rbf(__builtin_amdgcn_sqrtf(rbf(rbf(var) + 1e-5f))));
#pragma unroll
      for (int i = 0; i < 5; ++i) {
        v4u o;
#pragma unroll
        for (int k = 0; k < 4; ++k) o[k] = cvtpk(rbf(xf[i * 8 + 2 * k] - mb) * rden, rbf(xf[i * 8 + 2 * k + 1] - mb) * rden);
        if (i < 4 || n < 8) *(LAS v4u*)(L + L_XN + g * XN_STR + (n + 16 * i) * 16) = o;
      }
    }
    FL[F_TAG] = tag;
    asm volatile("" ::: "memory");
    FL[F_XN] = 1;
    unsigned t0;
    {
      const int off = WS_S1 + (pair >> 4) * S1_ROW + (pair & 15) * 1024 + lane * 16;
      const int offt = WS_T0 + (c & 15) * 128 + (lane & 7) * 16;
      v4u r, t;
#pragma nounroll
      for (unsigned s = 0; s < GLB_SPIN; ++s) {
        PLOAD(r, off, zero); PLOAD(t, offt, zero);
        asm volatile("s_waitcnt vmcnt(0)" : "+v"(r), "+v"(t) :: "memory");
        if (__all((r[1] == tag) & (r[3] == tag) & (t[1] == tag) & (t[3] == tag))) break;
      }
      float late = (float)(int)(t[0] - tstart);
      late = fmaxf(late, dpp<ROR4>(late)); late = fmaxf(late, dpp<ROR2>(late)); late = fmaxf(late, dpp<ROR1>(late));
      t0 = tstart + (unsigned)__builtin_amdgcn_readfirstlane((int)fminf(late, 200.f));
      *(LAS v2u*)(L + L_Q + lane * 8) = v2u{r[0], r[2]};
      asm volatile("" ::: "memory");
      FL[F_Q] = 1;
    }
    if (!isS) {
      const int rs = (c - N_S1) / 3, up = FF * 4;
      v4u G[5], U[5];
      int off[5], loff[5];
#pragma unroll
      for (int i = 0; i < 5; ++i) {
        const int t = min(lane + 64 * i, 287), b = t / 72, qd = t - b * 72;
        off[i] = WS_S1 + b * S1_ROW + (NQ + 288 * rs + 4 * qd) * 4; loff[i] = b * H_STR + qd * 8;
      }
#pragma nounroll
      for (unsigned s = 0; s < GLB_SPIN; ++s) {
#pragma unroll
        for (int i = 0; i < 5; ++i) { PLOAD(G[i], off[i], zero); PLOAD(U[i], off[i], up); }
        asm volatile("s_waitcnt vmcnt(0)" : "+v"(G[0]), "+v"(G[1]), "+v"(G[2]), "+v"(G[3]), "+v"(G[4]),
                     "+v"(U[0]), "+v"(U[1]), "+v"(U[2]), "+v"(U[3]), "+v"(U[4]) :: "memory");
        bool ok = true;
#pragma unroll
        for (int i = 0; i < 5; ++i) ok &= (G[i][1] == tag) & (G[i][3] == tag) & (U[i][1] == tag) & (U[i][3] == tag);
        if (__all(ok)) break;
        __builtin_amdgcn_s_sleep(4);
      }
      if (lane < 32) *(LAS v2u*)(L + L_H + (lane >> 3) * H_STR + 576 + (lane & 7) * 8) = v2u{0u, 0u};
#pragma unroll
      for (int i = 0; i < 5; ++i) {
        float hv[4];
#pragma unroll
        for (int k = 0; k < 4; ++k) {
          const unsigned gw = G[i][(k >> 1) * 2], uw = U[i][(k >> 1) * 2];
          const float a = (k & 1) ? bhi(gw) : blo(gw), u = (k & 1) ? bhi(uw) : blo(uw);
          const float z2 = (-2.f * 0.7978845608028654f * LOG2E) * (a + 0.044715f * a * a * a);
          hv[k] = rbf(a * rcp(1.f + ex2(z2))) * u;
        }
        *(LAS v2u*)(L + L_H + loff[i]) = v2u{cvtpk(hv[0], hv[1]), cvtpk(hv[2], hv[3])};
      }
      asm volatile("" ::: "memory");
      FL[F_H] = 1;
    }
    if (c >= N_WO) EXIT_WAVE
    const int fcs = c / 48, fr = c - fcs * 48, fw = fr >> 4, fpc = (fr >> 3) & 1, fgr = fr & 7;
    int oo = ((2 * (fgr & 1) + ((fgr >> 1) & 1)) * DM + 64 * (3 * fcs + fw) + 8 * (lane & 7) + 4 * (fgr >> 2) + 2 * fpc + (g & 1)) * 2;
    oo = (c < N_FIN && (lane & 40) == 0) ? oo : OOB;
    float xres = blo(__builtin_amdgcn_raw_buffer_load_b16(mkrsrc(x, 4 * DM * 2), oo, 0, 0));
    asm volatile("" : "+v"(xres), "+v"(oo));
    {
      const int hh = ksl >> 1, pc = (((n & 3) * 16 + (hh >> 1)) * 4 + 2 * (n >> 3));
      const int o1 = WS_H1 + pc * 1024 + (hh & 1) * 512 + (8 * (ksl & 1) + 4 * ((n >> 2) & 1) + g) * 32;
      unsigned p[12];
#pragma unroll
      for (int k = 0; k < 12; ++k) p[k] = 0u;
      lds_wait<1>(FL + F_PUB, 1);
      wait_until(max((int)((unsigned)__builtin_amdgcn_s_memrealtime() - t0) + D_AB, T_A), t0);
      asm volatile(
        "s_mov_b64 s[70:71], 0\n\t" "s_mov_b32 s72, %[n]\n\t"
        "v_cndmask_b32_e64 v119, %[o1], 2.0, s[70:71]\n\t"
        "buffer_load_dwordx4 v[120:123], v119, %[d], 0 offen offset:0 sc1\n\t"
        "buffer_load_dwordx4 v[124:127], v119, %[d], 0 offen offset:16 sc1\n\t"
        "buffer_load_dwordx4 v[128:131], v119, %[d], 0 offen offset:1024 sc1\n\t"
        "buffer_load_dwordx4 v[132:135], v119, %[d], 0 offen offset:1040 sc1\n\t"
        "s_sleep " STR(STAG) "\n\t"
        "1:\n\t"
        "v_cndmask_b32_e64 v119, %[o1], 2.0, s[70:71]\n\t"
        "buffer_load_dwordx4 v[136:139], v119, %[d], 0 offen offset:0 sc1\n\t"
        "buffer_load_dwordx4 v[140:143], v119, %[d], 0 offen offset:16 sc1\n\t"
        "buffer_load_dwordx4 v[144:147], v119, %[d], 0 offen offset:1024 sc1\n\t"
        "buffer_load_dwordx4 v[148:151], v119, %[d], 0 offen offset:1040 sc1\n\t"
        "s_waitcnt vmcnt(4)\n\t"
        "v_bitop3_b32 v112, v121, v123, %[t] bitop3:0x7e\n\t"
        "v_bitop3_b32 v113, v125, v127, %[t] bitop3:0x7e\n\t"
        "v_bitop3_b32 v114, v129, v131, %[t] bitop3:0x7e\n\t"
        "v_bitop3_b32 v115, v133, v135, %[t] bitop3:0x7e\n\t"
        "v_or3_b32 v112, v112, v113, v114\n\t" "v_or_b32 v112, v112, v115\n\t" "v_cmp_gt_u32 vcc, 0x10000, v112\n\t"
        "v_perm_b32 v113, v123, v121, %[sl]\n\t"
        "v_perm_b32 v114, v127, v125, %[sl]\n\t"
        "v_perm_b32 v115, v131, v129, %[sl]\n\t"
        "v_perm_b32 v116, v135, v133, %[sl]\n\t"
        "s_or_b64 s[70:71], s[70:71], vcc\n\t"
        "v_cndmask_b32 %[p0], %[p0], v120, vcc\n\t"
        "v_cndmask_b32 %[p1], %[p1], v122, vcc\n\t"
        "v_cndmask_b32 %[p2], %[p2], v124, vcc\n\t"
        "v_cndmask_b32 %[p3], %[p3], v126, vcc\n\t"
        "v_cndmask_b32 %[p4], %[p4], v128, vcc\n\t"
        "v_cndmask_b32 %[p5], %[p5], v130, vcc\n\t"
        "v_cndmask_b32 %[p6], %[p6], v132, vcc\n\t"
        "v_cndmask_b32 %[p7], %[p7], v134, vcc\n\t"
        "v_cndmask_b32 %[p8], %[p8], v113, vcc\n\t"
        "v_cndmask_b32 %[p9], %[p9], v114, vcc\n\t"
        "v_cndmask_b32 %[p10], %[p10], v115, vcc\n\t"
        "v_cndmask_b32 %[p11], %[p11], v116, vcc\n\t"
        "s_cmp_eq_u64 s[70:71], exec\n\t" "s_cbranch_scc1 2f\n\t"
        "v_cndmask_b32_e64 v119, %[o1], 2.0, s[70:71]\n\t"
        "buffer_load_dwordx4 v[120:123], v119, %[d], 0 offen offset:0 sc1\n\t"
        "buffer_load_dwordx4 v[124:127], v119, %[d], 0 offen offset:16 sc1\n\t"
        "buffer_load_dwordx4 v[128:131], v119, %[d], 0 offen offset:1024 sc1\n\t"
        "buffer_load_dwordx4 v[132:135], v119, %[d], 0 offen offset:1040 sc1\n\t"
        "s_waitcnt vmcnt(4)\n\t"
        "v_bitop3_b32 v112, v137, v139, %[t] bitop3:0x7e\n\t"
        "v_bitop3_b32 v113, v141, v143, %[t] bitop3:0x7e\n\t"
        "v_bitop3_b32 v114, v145, v147, %[t] bitop3:0x7e\n\t"
        "v_bitop3_b32 v115, v149, v151, %[t] bitop3:0x7e\n\t"
        "v_or3_b32 v112, v112, v113, v114\n\t" "v_or_b32 v112, v112, v115\n\t" "v_cmp_gt_u32 vcc, 0x10000, v112\n\t"
        "v_perm_b32 v113, v139, v137, %[sl]\n\t"
        "v_perm_b32 v114, v143, v141, %[sl]\n\t"
        "v_perm_b32 v115, v147, v145, %[sl]\n\t"
        "v_perm_b32 v116, v151, v149, %[sl]\n\t"
        "s_or_b64 s[70:71], s[70:71], vcc\n\t"
        "v_cndmask_b32 %[p0], %[p0], v136, vcc\n\t"
        "v_cndmask_b32 %[p1], %[p1], v138, vcc\n\t"
        "v_cndmask_b32 %[p2], %[p2], v140, vcc\n\t"
        "v_cndmask_b32 %[p3], %[p3], v142, vcc\n\t"
        "v_cndmask_b32 %[p4], %[p4], v144, vcc\n\t"
        "v_cndmask_b32 %[p5], %[p5], v146, vcc\n\t"
        "v_cndmask_b32 %[p6], %[p6], v148, vcc\n\t"
        "v_cndmask_b32 %[p7], %[p7], v150, vcc\n\t"
        "v_cndmask_b32 %[p8], %[p8], v113, vcc\n\t"
        "v_cndmask_b32 %[p9], %[p9], v114, vcc\n\t"
        "v_cndmask_b32 %[p10], %[p10], v115, vcc\n\t"
        "v_cndmask_b32 %[p11], %[p11], v116, vcc\n\t"
        "s_cmp_eq_u64 s[70:71], exec\n\t" "s_cbranch_scc1 2f\n\t"
        "s_bcnt0_i32_b64 s73, s[70:71]\n\t" "s_cmp_lt_u32 s73, 17\n\t" "s_cbranch_scc1 3f\n\t" "s_sleep 12\n\t" "3:\n\t"
        "s_sub_u32 s72, s72, 1\n\t" "s_cbranch_scc0 1b\n\t"
        "2:\n\t" "s_nop 1 ; POLL_END"
        : [p0] "+v"(p[0]), [p1] "+v"(p[1]), [p2] "+v"(p[2]), [p3] "+v"(p[3]), [p4] "+v"(p[4]), [p5] "+v"(p[5]), [p6] "+v"(p[6]), [p7] "+v"(p[7]), [p8] "+v"(p[8]), [p9] "+v"(p[9]), [p10] "+v"(p[10]), [p11] "+v"(p[11])
        : [o1] "v"(o1), [d] "s"(dws), [t] "v"(tag << 16), [sl] "s"(0x05040100u), [n] "i"(GLB_SPIN)
        : "memory", "vcc", "scc", "s70", "s71", "s72", "s73", "v112", "v113", "v114", "v115", "v116", "v117", "v118", "v119", "v120", "v121", "v122", "v123", "v124", "v125", "v126", "v127", "v128", "v129", "v130", "v131", "v132", "v133", "v134", "v135", "v136", "v137", "v138", "v139", "v140", "v141", "v142", "v143", "v144", "v145", "v146", "v147", "v148", "v149", "v150", "v151");
      const float ma = __uint_as_float(p[8]), mb = __uint_as_float(p[10]);
      float M = fmaxf(ma, mb); M = fmaxf(M, dpp<ROR8>(M));
      const float ea = __uint_as_float(p[9]) * ex2((ma - M) * QK_C), eb = __uint_as_float(p[11]) * ex2((mb - M) * QK_C);
      float Ls = ea + eb; Ls += dpp<ROR8>(Ls);
      const float inv = rcp(Ls);
      v4u a;
#pragma unroll
      for (int k = 0; k < 4; ++k) {
        float lo = fmaf(ea, blo(p[k]), eb * blo(p[4 + k])), hi = fmaf(ea, bhi(p[k]), eb * bhi(p[4 + k]));
        lo += dpp<ROR8>(lo); hi += dpp<ROR8>(hi);
        a[k] = cvtpk(lo * inv, hi * inv);
      }
      *(LAS v4u*)(L + L_AF + lane * 16) = a;
      asm volatile("" ::: "memory");
      FL[F_A] = 1;
      asm volatile("s_waitcnt vmcnt(0) ; POLL_DRAIN" ::: "memory");
    }
    if (c >= N_FIN) EXIT_WAVE
    const int lo_ = fw * 2048 + fpc * 1024 + fgr * 128 + (lane & 7) * 16;
    int o[9]; float pa[8], pb[8], f0 = 0.f, f1 = 0.f;
    o[8] = WS_F2 + (fcs * 16 + (lane >> 3)) * 6144 + lo_;
#pragma unroll
    for (int i = 0; i < 8; ++i) { o[i] = WS_H2 + (fcs * 64 + 8 * i + (lane >> 3)) * 6144 + lo_; pa[i] = 0.f; pb[i] = 0.f; }
    const __amdgpu_buffer_rsrc_t rout = mkrsrc(out, 4 * DM * 2);
    int oe = (c | lane) ? OOB : WS_EPOCH;
    asm volatile("" : "+v"(oe));
    lds_wait<0>(FL + F_H2, 3);
    wait_until(max((int)((unsigned)__builtin_amdgcn_s_memrealtime() - t0) + D_AB, T_B), t0);
    asm volatile(
        "s_mov_b32 s72, " STR(GLB_N) "\n\t" "s_mov_b32 s73, 0xc000\n\t"
        "buffer_load_dwordx4 v[88:91], %[o0], %[d], 0 offen sc1\n\t"
        "buffer_load_dwordx4 v[92:95], %[o1], %[d], 0 offen sc1\n\t"
        "buffer_load_dwordx4 v[96:99], %[o2], %[d], 0 offen sc1\n\t"
        "buffer_load_dwordx4 v[100:103], %[o3], %[d], 0 offen sc1\n\t"
        "buffer_load_dwordx4 v[104:107], %[o4], %[d], 0 offen sc1\n\t"
        "buffer_load_dwordx4 v[108:111], %[o5], %[d], 0 offen sc1\n\t"
        "buffer_load_dwordx4 v[112:115], %[o6], %[d], 0 offen sc1\n\t"
        "buffer_load_dwordx4 v[116:119], %[o7], %[d], 0 offen sc1\n\t"
        "buffer_load_dwordx4 v[120:123], %[o8], %[d], 0 offen sc1\n\t"
        "buffer_load_dwordx4 v[124:127], %[o8], %[d], s73 offen sc1\n\t"
        "s_sleep " STR(STAG) "\n\t"
        "1:\n\t"
        "buffer_load_dwordx4 v[128:131], %[o0], %[d], 0 offen sc1\n\t"
        "buffer_load_dwordx4 v[132:135], %[o1], %[d], 0 offen sc1\n\t"
        "buffer_load_dwordx4 v[136:139], %[o2], %[d], 0 offen sc1\n\t"
        "buffer_load_dwordx4 v[140:143], %[o3], %[d], 0 offen sc1\n\t"
        "buffer_load_dwordx4 v[144:147], %[o4], %[d], 0 offen sc1\n\t"
        "buffer_load_dwordx4 v[148:151], %[o5], %[d], 0 offen sc1\n\t"
        "buffer_load_dwordx4 v[152:155], %[o6], %[d], 0 offen sc1\n\t"
        "buffer_load_dwordx4 v[156:159], %[o7], %[d], 0 offen sc1\n\t"
        "buffer_load_dwordx4 v[160:163], %[o8], %[d], 0 offen sc1\n\t"
        "buffer_load_dwordx4 v[164:167], %[o8], %[d], s73 offen sc1\n\t"
        "s_waitcnt vmcnt(10)\n\t"
        "v_bitop3_b32 v78, v89, v91, %[t] bitop3:0x7e\n\t"
        "v_bitop3_b32 v79, v93, v95, %[t] bitop3:0x7e\n\t"
        "v_bitop3_b32 v80, v97, v99, %[t] bitop3:0x7e\n\t"
        "v_bitop3_b32 v81, v101, v103, %[t] bitop3:0x7e\n\t"
        "v_bitop3_b32 v82, v105, v107, %[t] bitop3:0x7e\n\t"
        "v_bitop3_b32 v83, v109, v111, %[t] bitop3:0x7e\n\t"
        "v_bitop3_b32 v84, v113, v115, %[t] bitop3:0x7e\n\t"
        "v_bitop3_b32 v85, v117, v119, %[t] bitop3:0x7e\n\t"
        "v_bitop3_b32 v86, v121, v123, %[t] bitop3:0x7e\n\t"
        "v_bitop3_b32 v87, v125, v127, %[t] bitop3:0x7e\n\t"
        "v_or_b32 v86, v86, v87\n\t"
        "v_cmp_eq_u32 s[74:75], 0, v78\n\t"
        "v_cmp_eq_u32 s[76:77], 0, v79\n\t"
        "v_cmp_eq_u32 s[78:79], 0, v80\n\t"
        "v_cmp_eq_u32 s[80:81], 0, v81\n\t"
        "v_cmp_eq_u32 s[82:83], 0, v82\n\t"
        "v_cmp_eq_u32 s[84:85], 0, v83\n\t"
        "v_cmp_eq_u32 s[86:87], 0, v84\n\t"
        "v_cmp_eq_u32 s[88:89], 0, v85\n\t"
        "v_cmp_eq_u32 s[90:91], 0, v86\n\t"
        "v_cndmask_b32_e64 %[o0], %[o0], 2.0, s[74:75]\n\t"
        "v_cndmask_b32_e64 %[o1], %[o1], 2.0, s[76:77]\n\t"
        "v_cndmask_b32_e64 %[o2], %[o2], 2.0, s[78:79]\n\t"
        "v_cndmask_b32_e64 %[o3], %[o3], 2.0, s[80:81]\n\t"
        "v_cndmask_b32_e64 %[o4], %[o4], 2.0, s[82:83]\n\t"
        "v_cndmask_b32_e64 %[o5], %[o5], 2.0, s[84:85]\n\t"
        "v_cndmask_b32_e64 %[o6], %[o6], 2.0, s[86:87]\n\t"
        "v_cndmask_b32_e64 %[o7], %[o7], 2.0, s[88:89]\n\t"
        "v_cndmask_b32_e64 %[o8], %[o8], 2.0, s[90:91]\n\t"
        "v_min3_u32 v78, %[o0], %[o1], %[o2]\n\t" "v_min3_u32 v79, %[o3], %[o4], %[o5]\n\t" "v_min3_u32 v80, %[o6], %[o7], %[o8]\n\t" "v_min3_u32 v78, v78, v79, v80\n\t"
        "v_cmp_eq_u32 vcc, 2.0, v78\n\t"
        "v_add_f32 v79, v120, v124\n\t" "v_add_f32 v80, v122, v126\n\t"
        "v_cndmask_b32_e64 %[a0], %[a0], v88, s[74:75]\n\t" "v_cndmask_b32_e64 %[b0], %[b0], v90, s[74:75]\n\t"
        "v_cndmask_b32_e64 %[a1], %[a1], v92, s[76:77]\n\t" "v_cndmask_b32_e64 %[b1], %[b1], v94, s[76:77]\n\t"
        "v_cndmask_b32_e64 %[a2], %[a2], v96, s[78:79]\n\t" "v_cndmask_b32_e64 %[b2], %[b2], v98, s[78:79]\n\t"
        "v_cndmask_b32_e64 %[a3], %[a3], v100, s[80:81]\n\t" "v_cndmask_b32_e64 %[b3], %[b3], v102, s[80:81]\n\t"
        "v_cndmask_b32_e64 %[a4], %[a4], v104, s[82:83]\n\t" "v_cndmask_b32_e64 %[b4], %[b4], v106, s[82:83]\n\t"
        "v_cndmask_b32_e64 %[a5], %[a5], v108, s[84:85]\n\t" "v_cndmask_b32_e64 %[b5], %[b5], v110, s[84:85]\n\t"
        "v_cndmask_b32_e64 %[a6], %[a6], v112, s[86:87]\n\t" "v_cndmask_b32_e64 %[b6], %[b6], v114, s[86:87]\n\t"
        "v_cndmask_b32_e64 %[a7], %[a7], v116, s[88:89]\n\t" "v_cndmask_b32_e64 %[b7], %[b7], v118, s[88:89]\n\t"
        "v_cndmask_b32_e64 %[f0], %[f0], v79, s[90:91]\n\t" "v_cndmask_b32_e64 %[f1], %[f1], v80, s[90:91]\n\t"
        "s_cmp_eq_u64 vcc, exec\n\t" "s_cbranch_scc1 2f\n\t"
        "buffer_load_dwordx4 v[88:91], %[o0], %[d], 0 offen sc1\n\t"
        "buffer_load_dwordx4 v[92:95], %[o1], %[d], 0 offen sc1\n\t"
        "buffer_load_dwordx4 v[96:99], %[o2], %[d], 0 offen sc1\n\t"
        "buffer_load_dwordx4 v[100:103], %[o3], %[d], 0 offen sc1\n\t"
        "buffer_load_dwordx4 v[104:107], %[o4], %[d], 0 offen sc1\n\t"
        "buffer_load_dwordx4 v[108:111], %[o5], %[d], 0 offen sc1\n\t"
        "buffer_load_dwordx4 v[112:115], %[o6], %[d], 0 offen sc1\n\t"
        "buffer_load_dwordx4 v[116:119], %[o7], %[d], 0 offen sc1\n\t"
        "buffer_load_dwordx4 v[120:123], %[o8], %[d], 0 offen sc1\n\t"
        "buffer_load_dwordx4 v[124:127], %[o8], %[d], s73 offen sc1\n\t"
        "s_waitcnt vmcnt(10)\n\t"
        "v_bitop3_b32 v78, v129, v131, %[t] bitop3:0x7e\n\t"
        "v_bitop3_b32 v79, v133, v135, %[t] bitop3:0x7e\n\t"
        "v_bitop3_b32 v80, v137, v139, %[t] bitop3:0x7e\n\t"
        "v_bitop3_b32 v81, v141, v143, %[t] bitop3:0x7e\n\t"
        "v_bitop3_b32 v82, v145, v147, %[t] bitop3:0x7e\n\t"
        "v_bitop3_b32 v83, v149, v151, %[t] bitop3:0x7e\n\t"
        "v_bitop3_b32 v84, v153, v155, %[t] bitop3:0x7e\n\t"
        "v_bitop3_b32 v85, v157, v159, %[t] bitop3:0x7e\n\t"
        "v_bitop3_b32 v86, v161, v163, %[t] bitop3:0x7e\n\t"
        "v_bitop3_b32 v87, v165, v167, %[t] bitop3:0x7e\n\t"
        "v_or_b32 v86, v86, v87\n\t"
        "v_cmp_eq_u32 s[74:75], 0, v78\n\t"
        "v_cmp_eq_u32 s[76:77], 0, v79\n\t"
        "v_cmp_eq_u32 s[78:79], 0, v80\n\t"
        "v_cmp_eq_u32 s[80:81], 0, v81\n\t"
        "v_cmp_eq_u32 s[82:83], 0, v82\n\t"
        "v_cmp_eq_u32 s[84:85], 0, v83\n\t"
        "v_cmp_eq_u32 s[86:87], 0, v84\n\t"
        "v_cmp_eq_u32 s[88:89], 0, v85\n\t"
        "v_cmp_eq_u32 s[90:91], 0, v86\n\t"
        "v_cndmask_b32_e64 %[o0], %[o0], 2.0, s[74:75]\n\t"
        "v_cndmask_b32_e64 %[o1], %[o1], 2.0, s[76:77]\n\t"
        "v_cndmask_b32_e64 %[o2], %[o2], 2.0, s[78:79]\n\t"
        "v_cndmask_b32_e64 %[o3], %[o3], 2.0, s[80:81]\n\t"
        "v_cndmask_b32_e64 %[o4], %[o4], 2.0, s[82:83]\n\t"
        "v_cndmask_b32_e64 %[o5], %[o5], 2.0, s[84:85]\n\t"
        "v_cndmask_b32_e64 %[o6], %[o6], 2.0, s[86:87]\n\t"
        "v_cndmask_b32_e64 %[o7], %[o7], 2.0, s[88:89]\n\t"
        "v_cndmask_b32_e64 %[o8], %[o8], 2.0, s[90:91]\n\t"
        "v_min3_u32 v78, %[o0], %[o1], %[o2]\n\t" "v_min3_u32 v79, %[o3], %[o4], %[o5]\n\t" "v_min3_u32 v80, %[o6], %[o7], %[o8]\n\t" "v_min3_u32 v78, v78, v79, v80\n\t"
        "v_cmp_eq_u32 vcc, 2.0, v78\n\t"
        "v_add_f32 v79, v160, v164\n\t" "v_add_f32 v80, v162, v166\n\t"
        "v_cndmask_b32_e64 %[a0], %[a0], v128, s[74:75]\n\t" "v_cndmask_b32_e64 %[b0], %[b0], v130, s[74:75]\n\t"
        "v_cndmask_b32_e64 %[a1], %[a1], v132, s[76:77]\n\t" "v_cndmask_b32_e64 %[b1], %[b1], v134, s[76:77]\n\t"
        "v_cndmask_b32_e64 %[a2], %[a2], v136, s[78:79]\n\t" "v_cndmask_b32_e64 %[b2], %[b2], v138, s[78:79]\n\t"
        "v_cndmask_b32_e64 %[a3], %[a3], v140, s[80:81]\n\t" "v_cndmask_b32_e64 %[b3], %[b3], v142, s[80:81]\n\t"
        "v_cndmask_b32_e64 %[a4], %[a4], v144, s[82:83]\n\t" "v_cndmask_b32_e64 %[b4], %[b4], v146, s[82:83]\n\t"
        "v_cndmask_b32_e64 %[a5], %[a5], v148, s[84:85]\n\t" "v_cndmask_b32_e64 %[b5], %[b5], v150, s[84:85]\n\t"
        "v_cndmask_b32_e64 %[a6], %[a6], v152, s[86:87]\n\t" "v_cndmask_b32_e64 %[b6], %[b6], v154, s[86:87]\n\t"
        "v_cndmask_b32_e64 %[a7], %[a7], v156, s[88:89]\n\t" "v_cndmask_b32_e64 %[b7], %[b7], v158, s[88:89]\n\t"
        "v_cndmask_b32_e64 %[f0], %[f0], v79, s[90:91]\n\t" "v_cndmask_b32_e64 %[f1], %[f1], v80, s[90:91]\n\t"
        "s_cmp_eq_u64 vcc, exec\n\t" "s_cbranch_scc1 2f\n\t"
        "s_sub_u32 s72, s72, 1\n\t" "s_cbranch_scc0 1b\n\t"
        "2:\n\t" "s_nop 1 ; POLL_END"
        : [o0] "+v"(o[0]), [o1] "+v"(o[1]), [o2] "+v"(o[2]), [o3] "+v"(o[3]), [o4] "+v"(o[4]), [o5] "+v"(o[5]), [o6] "+v"(o[6]), [o7] "+v"(o[7]), [o8] "+v"(o[8]),
          [a0] "+v"(pa[0]), [a1] "+v"(pa[1]), [a2] "+v"(pa[2]), [a3] "+v"(pa[3]), [a4] "+v"(pa[4]), [a5] "+v"(pa[5]), [a6] "+v"(pa[6]), [a7] "+v"(pa[7]),
          [b0] "+v"(pb[0]), [b1] "+v"(pb[1]), [b2] "+v"(pb[2]), [b3] "+v"(pb[3]), [b4] "+v"(pb[4]), [b5] "+v"(pb[5]), [b6] "+v"(pb[6]), [b7] "+v"(pb[7]), [f0] "+v"(f0), [f1] "+v"(f1)
        : [d] "s"(dws), [t] "v"(tag)
        : "memory", "vcc", "scc", "s72", "s73", "s74", "s75", "s76", "s77", "s78", "s79", "s80", "s81", "s82", "s83", "s84", "s85", "s86", "s87", "s88", "s89", "s90", "s91", "v78", "v79", "v80", "v81", "v82", "v83", "v84", "v85", "v86", "v87", "v88", "v89", "v90", "v91", "v92", "v93", "v94", "v95", "v96", "v97", "v98", "v99", "v100", "v101", "v102", "v103", "v104", "v105", "v106", "v107", "v108", "v109", "v110", "v111", "v112", "v113", "v114", "v115", "v116", "v117", "v118", "v119", "v120", "v121", "v122", "v123", "v124", "v125", "v126", "v127", "v128", "v129", "v130", "v131", "v132", "v133", "v134", "v135", "v136", "v137", "v138", "v139", "v140", "v141", "v142", "v143", "v144", "v145", "v146", "v147", "v148", "v149", "v150", "v151", "v152", "v153", "v154", "v155", "v156", "v157", "v158", "v159", "v160", "v161", "v162", "v163", "v164", "v165", "v166", "v167");
    const float s0 = ((pa[0] + pa[1]) + (pa[2] + pa[3])) + ((pa[4] + pa[5]) + (pa[6] + pa[7]));
    const float s1 = ((pb[0] + pb[1]) + (pb[2] + pb[3])) + ((pb[4] + pb[5]) + (pb[6] + pb[7]));
    const float f = rbf(pair_sum(f0, f1)), gate = rbf(f * rcp(1.f + ex2(-LOG2E * f)));
    const float res = xres + rbf(rbf(pair_sum(s0, s1)) * gate);
    __builtin_amdgcn_raw_buffer_store_b16((u16)cvtpk(res, res), rout, oo, 0, 0);
    __builtin_amdgcn_raw_buffer_store_b32(tag, rws, oe, 0, SC1);
    asm volatile("s_waitcnt vmcnt(0) ; POLL_DRAIN" ::: "memory");
    EXIT_WAVE
  }

  LANE_IDS
  const int w = wave;
  __builtin_amdgcn_s_setprio(2);
  const int rowl = 32 * (n >> 3) + 8 * g, chk = (n & 7) * 16;
  const int afo = ((n >> 2) & 1) * 64 + g * 16;
  f4 acc[8];
#pragma unroll
  for (int j = 0; j < 8; ++j) acc[j] = f4{0.f, 0.f, 0.f, 0.f};
  if (__builtin_expect(isS, 1)) {
    const u16* base = c < 64 ? wq + c * 64 : c < 136 ? wg + (c - 64) * 64 : wu + (c - 136) * 64;
    const int stride = c < 64 ? NQ * 2 : FF * 2;
    const __amdgpu_buffer_rsrc_t rw = mkrsrc(base, (DM - 1) * stride + 128);
    const int voff = rowl * stride + chk;
    const unsigned tbase = tstart + (unsigned)(((2 * w - AHEAD) * TAU) >> 8);
    v4u R[24];
#pragma unroll
    for (int i = 0; i < 24; ++i) {
      if ((i & 1) == 0 && 3 * i > AHEAD) PACE(3 * i);
      R[i] = ld_nt(rw, voff, (192 * w + 64 * (i >> 3) + (i & 7)) * stride);
    }
    __builtin_amdgcn_sched_barrier(0);
    lds_wait<1>(FL + F_XN, 1);
#pragma unroll
    for (int gi = 0; gi < 3; ++gi)
      gemv_group(R + 8 * gi, *(const LAS v4u*)(L + L_XN + (n & 3) * XN_STR + (3 * w + gi) * 128 + afo), acc);
    *(LAS f4*)(L + L_S1R + w * 1024 + lane * 16) = gemv_finish(acc, lane);
    const unsigned tag = FL[F_TAG];
    if (lds_arrive(F_S1) == 2) {
      const f4 o = (*(const LAS f4*)(L + L_S1R + lane * 16) + *(const LAS f4*)(L + L_S1R + 1024 + lane * 16)) + *(const LAS f4*)(L + L_S1R + 2048 + lane * 16);
      st_sc1(v4u{cvtpk(o[0], o[1]), tag, cvtpk(o[2], o[3]), tag}, rws,
             WS_S1 + (2 * (n >> 3) + (g & 1)) * S1_ROW + (64 * c + 8 * (n & 7) + 4 * (g >> 1)) * 4);
    }
    if (c >= N_WO) EXIT_WAVE
    lds_wait<1>(FL + F_D, WOCNT);
    const __amdgpu_buffer_rsrc_t ro = mkrsrc(wo + (size_t)ksl * 64 * DM + (3 * cs + w) * 64, 63 * DM * 2 + 128);
    v4u W[8], T[8];
#pragma unroll
    for (int i = 0; i < 8; ++i) W[i] = ld_nt(ro, rowl * (DM * 2) + chk + i * (DM * 2));
#pragma unroll
    for (int q = 0; q < 4; ++q)
#pragma unroll
      for (int p = 0; p < 4; ++p) {
        T[2 * q][p] = __builtin_amdgcn_perm(W[2 * p + 1][q], W[2 * p][q], 0x05040100u);
        T[2 * q + 1][p] = __builtin_amdgcn_perm(W[2 * p + 1][q], W[2 * p][q], 0x07060302u);
      }
    int rec = WS_H2 + (cs * 64 + ksl) * 6144 + w * 2048 + lane * 16;
    asm volatile("" : "+v"(T[0]), "+v"(T[1]), "+v"(T[2]), "+v"(T[3]), "+v"(T[4]), "+v"(T[5]), "+v"(T[6]), "+v"(T[7]), "+v"(rec));
    v4u alo, ahi;
    {
      const int af = L_AF + lane * 16;
      unsigned f_, s_;
      asm volatile("s_mov_b32 s72, %7\n1:\n\tds_read_b32 %2, %4\n\tds_read_b128 %0, %5\n\tds_read_b128 %1, %6\n\ts_waitcnt lgkmcnt(2)\n\t"
                   "v_readfirstlane_b32 %3, %2\n\ts_cmp_lg_u32 %3, 0\n\ts_cbranch_scc1 2f\n\ts_sub_u32 s72, s72, 1\n\ts_cbranch_scc0 1b\n2:\n\ts_waitcnt lgkmcnt(0)"
                   : "=&v"(alo), "=&v"(ahi), "=&v"(f_), "=&s"(s_)
                   : "v"(L_FLAG + 4 * F_A), "v"(n < 8 ? af : L_ZERO), "v"(n < 8 ? L_ZERO : af), "i"(LDS_SPIN) : "memory", "scc", "s72");
    }
#pragma unroll
    for (int j = 0; j < 4; ++j) acc[j] = mfma(alo, T[j], f4{0.f, 0.f, 0.f, 0.f});
#pragma unroll
    for (int j = 0; j < 4; ++j) acc[j] = mfma(ahi, T[j + 4], acc[j]);
    const f4 G = gemv_fold(acc, lane);
    st_sc1(v4u{__float_as_uint(G[0]), tag, __float_as_uint(G[1]), tag}, rws, rec);
    st_sc1(v4u{__float_as_uint(G[2]), tag, __float_as_uint(G[3]), tag}, rws, rec + 1024);
    lds_post(F_H2);
  } else {
    const int ds = (c - N_S1) % 3, rs = (c - N_S1) / 3;
    const __amdgpu_buffer_rsrc_t rd = mkrsrc(wd + (size_t)rs * 288 * DM + (3 * ds + w) * 64, 287 * DM * 2 + 128);
    const int voff = rowl * (DM * 2) + chk, voff4 = n >= 8 ? OOB : voff;
    v4u R[24];
#pragma unroll
    for (int i = 0; i < 8; ++i) R[i] = ld_nt(rd, voff, i * (DM * 2));
    __builtin_amdgcn_sched_barrier(0);
    lds_wait<1>(FL + F_ISS, 8);
    const unsigned tb2 = (unsigned)__builtin_amdgcn_s_memrealtime() + (unsigned)((2 * w * TAUD) >> 8);
#define PACE_D(i) if (((i) & 1) == 0 && 3 * (i) > AHD) wait_until((int)(((3 * (i) - AHD) * TAUD) >> 8), tb2);
#pragma unroll
    for (int i = 8; i < 24; ++i) { PACE_D(i - 8) R[i] = ld_nt(rd, voff, (64 * (i >> 3) + (i & 7)) * (DM * 2)); }
    __builtin_amdgcn_sched_barrier(0);
    lds_wait<1>(FL + F_H, 1);
    const unsigned tag = FL[F_TAG];
    const int ao = L_H + (n & 3) * H_STR + afo;
    gemv_group(R, *(const LAS v4u*)(L + ao), acc);
    __builtin_amdgcn_sched_barrier(0);
#pragma unroll
    for (int i = 0; i < 8; ++i) { PACE_D(16 + i) R[i] = ld_nt(rd, voff, (192 + i) * (DM * 2)); }
    __builtin_amdgcn_sched_barrier(0);
    gemv_group(R + 8, *(const LAS v4u*)(L + ao + 128), acc);
    __builtin_amdgcn_sched_barrier(0);
#pragma unroll
    for (int i = 0; i < 8; ++i) R[8 + i] = ld_nt(rd, voff4, (256 + i) * (DM * 2));
    __builtin_amdgcn_sched_barrier(0);
    gemv_group(R + 16, *(const LAS v4u*)(L + ao + 256), acc);
    gemv_group(R, *(const LAS v4u*)(L + ao + 384), acc);
    gemv_group(R + 8, *(const LAS v4u*)(L + ao + 512), acc);
    const f4 G = gemv_finish(acc, lane);
    const int rec = WS_F2 + (ds * 16 + rs) * 6144 + w * 2048 + lane * 16;
    st_sc1(v4u{__float_as_uint(G[0]), tag, __float_as_uint(G[1]), tag}, rws, rec);
    st_sc1(v4u{__float_as_uint(G[2]), tag, __float_as_uint(G[3]), tag}, rws, rec + 1024);
  }
}

extern "C" void kernel_launch(void* const* d_in, const int* in_sizes, int n_in, void* d_out, int out_size, void* d_ws,
                              size_t ws_size, hipStream_t stream) {
  fused_decode<<<NCU, NTHR, 0, stream>>>((const u16*)d_in[0], (const u16*)d_in[1], (const u16*)d_in[2], (const u16*)d_in[3],
                                         (const u16*)d_in[4], (const u16*)d_in[6], (const u16*)d_in[7], (const u16*)d_in[8],
                                         (u16*)d_out, (unsigned char*)d_ws);
}
```

```cpp
#include <hip/hip_runtime.h>
#include <stdint.h>

typedef unsigned short u16;
typedef unsigned v4u __attribute__((ext_vector_type(4)));
typedef unsigned v2u __attribute__((ext_vector_type(2)));
typedef float f4 __attribute__((ext_vector_type(4)));
typedef short s8v __attribute__((ext_vector_type(8)));
typedef __bf16 bf2v __attribute__((ext_vector_type(2)));
#define LAS __attribute__((address_space(3)))
#define STR2(x) #x
#define STR(x) STR2(x)

constexpr int DM = 576, SK = 8192, WIN = 2048, FF = 4608, NQ = 4096;
constexpr int NCU = 256, NTHR = 768;
constexpr int N_S1 = 208, N_WO = 192, N_FIN = 144;
constexpr int OOB = 0x40000000;
constexpr int NT = 2, SC1 = 16;
constexpr float LOG2E = 1.4426950408889634f, QK_SCALE = 0.08838834764831845f;

#define TAU 1016
#define AHEAD 28
#define TAUD 850
#define AHD 12
#define DSTEP 10u
#define T_A 1356
#define T_B 1484
#define D_AB 26
#define CATCH 50
#define VSPLIT 10
#define WOCNT 4
#define STAG 6
#define CODEPF 21504

constexpr int S1_ROW = (NQ + 2 * FF) * 4;
constexpr int WS_EPOCH = 0, WS_T0 = 1024, WS_S1 = 4096, WS_H1 = WS_S1 + 4 * S1_ROW, WS_H2 = WS_H1 + NCU * 1024,
              WS_F2 = WS_H2 + 3 * 64 * 6144, WS_END = WS_F2 + 3 * 16 * 6144;
constexpr int XN_STR = 1168, H_STR = 656;
constexpr int L_XN = 0, L_H = L_XN + 4 * XN_STR, L_Q = L_H + 4 * H_STR, L_S1R = L_Q + 512, L_AM = L_S1R + 3 * 1024,
              L_AL = L_AM + 64, L_AD = L_AL + 64, L_AF = L_AD + 8 * 1024, L_XS = L_AF + 1024, L_DUM = L_XS + 5376,
              L_ZERO = L_DUM + 1024, L_FLAG = L_ZERO + 256, L_END = L_FLAG + 64;
enum { F_XN = 0, F_TAG, F_Q, F_H, F_S1, F_ISS, F_D, F_PUB, F_L, F_A, F_H2, F_SH };
static_assert(L_H % 16 == 0 && L_Q % 16 == 0 && L_S1R % 16 == 0 && L_AM % 16 == 0 && L_AD % 16 == 0 && L_XS % 16 == 0 &&
              L_DUM % 16 == 0 && L_ZERO % 16 == 0, "align");
#define GLB_N 262144
constexpr unsigned LDS_SPIN = 1u << 22, GLB_SPIN = GLB_N;

#define PACE(kb) asm volatile("1:\n\ts_memrealtime s[92:93]\n\ts_waitcnt lgkmcnt(0)\n\ts_sub_u32 s92, s92, %0\n\ts_cmp_lt_i32 s92, %1\n\t" \
    "s_cbranch_scc0 2f\n\ts_sleep 1\n\ts_branch 1b\n2:" :: "s"(tbase), "i"((int)(((kb) * TAU) >> 8)) : "scc", "memory", "s92", "s93")
#define PACE_A(kb) asm volatile("1:\n\ts_memrealtime s[92:93]\n\ts_waitcnt lgkmcnt(0)\n\ts_sub_u32 s92, s92, %0\n\ts_cmp_lt_i32 s92, %1\n\t" \
    "s_cbranch_scc0 2f\n\ts_sleep 1\n\ts_branch 1b\n2:\n\ts_add_u32 %0, %0, %2" : "+s"(tbase) : "i"((int)(((kb) * TAU) >> 8)), "s"(dstep) : "scc", "memory", "s92", "s93")
#define PLOAD(d, voff, soff) asm volatile("buffer_load_dwordx4 %0, %1, %2, %3 offen sc1" : "=v"(d) : "v"(voff), "s"(dws), "s"(soff) : "memory")
#define EXIT_WAVE { __builtin_amdgcn_endpgm(); __builtin_unreachable(); }

__device__ __forceinline__ __amdgpu_buffer_rsrc_t mkrsrc(const void* p, unsigned bytes) {
  return __builtin_amdgcn_make_buffer_rsrc((void*)p, 0, bytes, 0x00020000);
}
__device__ __forceinline__ v4u ld_nt(__amdgpu_buffer_rsrc_t r, int voff, int soff = 0) {
  return __builtin_amdgcn_raw_buffer_load_b128(r, voff, soff, NT);
}
__device__ __forceinline__ void st_sc1(v4u v, __amdgpu_buffer_rsrc_t r, int voff) {
  __builtin_amdgcn_raw_buffer_store_b128(v, r, voff, 0, SC1);
}
__device__ __forceinline__ f4 mfma(v4u a, v4u b, f4 c) {
  return __builtin_amdgcn_mfma_f32_16x16x32_bf16(__builtin_bit_cast(s8v, a), __builtin_bit_cast(s8v, b), c, 0, 0, 0);
}
__device__ __forceinline__ unsigned cvtpk(float lo, float hi) {
  bf2v r = {(__bf16)lo, (__bf16)hi};
  return __builtin_bit_cast(unsigned, r);
}
__device__ __forceinline__ float rbf(float f) { return (float)(__bf16)f; }
__device__ __forceinline__ float rcp(float f) { return __builtin_amdgcn_rcpf(f); }
__device__ __forceinline__ float ex2(float f) { return __builtin_amdgcn_exp2f(f); }
__device__ __forceinline__ float blo(unsigned u) { return __uint_as_float(u << 16); }
__device__ __forceinline__ float bhi(unsigned u) { return __uint_as_float(u & 0xffff0000u); }
__device__ __forceinline__ unsigned mktag(unsigned e) { return ((e + 1u) & 0xffffu) ? e + 1u : e + 2u; }
template <int CTRL> __device__ __forceinline__ float dpp(float x) {
  return __builtin_bit_cast(float, __builtin_amdgcn_mov_dpp(__builtin_bit_cast(int, x), CTRL, 0xf, 0xf, true));
}
constexpr int ROR1 = 0x121, ROR2 = 0x122, ROR4 = 0x124, ROR8 = 0x128, XOR1 = 0xB1;
__device__ __forceinline__ float row_sum(float x) { x += dpp<ROR8>(x); x += dpp<ROR4>(x); x += dpp<ROR2>(x); x += dpp<ROR1>(x); return x; }
__device__ __forceinline__ float row_max(float x) {
  x = fmaxf(x, dpp<ROR8>(x)); x = fmaxf(x, dpp<ROR4>(x)); x = fmaxf(x, dpp<ROR2>(x)); x = fmaxf(x, dpp<ROR1>(x)); return x; }
__device__ __forceinline__ void swap16(float& a, float& b) {
  auto s = __builtin_amdgcn_permlane16_swap(__float_as_uint(a), __float_as_uint(b), false, false);
  a = __uint_as_float(s[0]); b = __uint_as_float(s[1]);
}
__device__ __forceinline__ void swap32(float& a, float& b) {
  auto s = __builtin_amdgcn_permlane32_swap(__float_as_uint(a), __float_as_uint(b), false, false);
  a = __uint_as_float(s[0]); b = __uint_as_float(s[1]);
}
__device__ __forceinline__ float xrow_sum(float x) { float a = x, b = x; swap16(a, b); x = a + b; a = x; b = x; swap32(a, b); return a + b; }
__device__ __forceinline__ float xrow_max(float x) { float a = x, b = x; swap16(a, b); x = fmaxf(a, b); a = x; b = x; swap32(a, b); return fmaxf(a, b); }

template <int SLEEP> __device__ __forceinline__ void lds_wait(volatile LAS unsigned* f, unsigned want) {
#pragma nounroll
  for (unsigned s = 0; s < LDS_SPIN; ++s) {
    if ((unsigned)__builtin_amdgcn_readfirstlane(*f) >= want) break;
    if (SLEEP) __builtin_amdgcn_s_sleep(SLEEP);
  }
  asm volatile("" ::: "memory");
}
__device__ __forceinline__ void wait_until(int ticks, unsigned t0) {
  asm volatile("1:\n\ts_memrealtime s[92:93]\n\ts_waitcnt lgkmcnt(0)\n\ts_sub_u32 s92, s92, %0\n\ts_cmp_lt_i32 s92, %1\n\ts_cbranch_scc1 1b"
               :: "s"(t0), "s"(ticks) : "scc", "memory", "s92", "s93");
}
__device__ __forceinline__ void lds_post(int flag) {
  asm volatile("s_mov_b64 exec, 1\n\tds_add_u32 %0, %1\n\ts_mov_b64 exec, -1" :: "v"(L_FLAG + 4 * flag), "v"(1u) : "memory");
}
__device__ __forceinline__ unsigned lds_arrive(int flag) {
  unsigned o, r;
  asm volatile("s_mov_b64 exec, 1\n\tds_add_rtn_u32 %0, %2, %3\n\ts_mov_b64 exec, -1\n\ts_waitcnt lgkmcnt(0)\n\tv_readfirstlane_b32 %1, %0"
               : "=&v"(o), "=s"(r) : "v"(L_FLAG + 4 * flag), "v"(1u) : "memory");
  return r;
}

__device__ __forceinline__ void gemv_group(const v4u* r, v4u a, f4* acc) {
#pragma unroll
  for (int w = 0; w < 4; ++w) {
    v4u lo, hi;
#pragma unroll
    for (int p = 0; p < 4; ++p) {
      lo[p] = __builtin_amdgcn_perm(r[2 * p + 1][w], r[2 * p][w], 0x05040100u);
      hi[p] = __builtin_amdgcn_perm(r[2 * p + 1][w], r[2 * p][w], 0x07060302u);
    }
    acc[2 * w] = mfma(a, lo, acc[2 * w]);
    acc[2 * w + 1] = mfma(a, hi, acc[2 * w + 1]);
  }
}
__device__ __forceinline__ f4 gemv_fold(const f4* e, int lane) {
  f4 G;
#pragma unroll
  for (int j = 0; j < 4; ++j) {
    float e0 = e[j][0], e1 = e[j][1], e2 = e[j][2], e3 = e[j][3];
    swap16(e0, e1); swap16(e2, e3);
    const float t0 = e0 + dpp<ROR8>(e1), t1 = dpp<ROR8>(e2) + e3;
    G[j] = (lane & 8) ? t1 : t0;
  }
  return G;
}
__device__ __forceinline__ f4 gemv_fold2(const f4* e, int lane) {
  f4 G;
#pragma unroll
  for (int i = 0; i < 2; ++i)
#pragma unroll
    for (int j = 0; j < 2; ++j) {
      float e0 = e[j][i], e1 = e[j + 2][i], e2 = e[j][2 + i], e3 = e[j + 2][2 + i];
      swap16(e0, e1); swap16(e2, e3);
      const float t0 = e0 + dpp<ROR8>(e1), t1 = dpp<ROR8>(e2) + e3;
      G[2 * i + j] = (lane & 8) ? t1 : t0;
    }
  return G;
}
template <bool REC> __device__ __forceinline__ f4 gemv_finish(const f4* acc, int lane) {
  f4 e[4];
#pragma unroll
  for (int j = 0; j < 4; ++j) e[j] = lane < 32 ? acc[j] : acc[j + 4];
  return REC ? gemv_fold2(e, lane) : gemv_fold(e, lane);
}

__global__ void __launch_bounds__(NTHR, 3)
fused_decode(const u16* __restrict__ x, const u16* __restrict__ kc, const u16* __restrict__ vc, const u16* __restrict__ wq,
             const u16* __restrict__ wo, const u16* __restrict__ wg, const u16* __restrict__ wu, const u16* __restrict__ wd,
             u16* __restrict__ out, unsigned char* ws) {
  __shared__ __attribute__((aligned(16))) unsigned char lds_raw[L_END];
  LAS unsigned char* L = (LAS unsigned char*)lds_raw;
  volatile LAS unsigned* FL = (volatile LAS unsigned*)(L + L_FLAG);
  const int c = blockIdx.x;
  const bool isS = c < N_S1;
  const int pair = (c & 7) * 8 + (c >> 5), sp = (c >> 3) & 3;
  int ksl = (c & 7) * 8 + (c >> 3) / 3, cs = (c >> 3) % 3;
  asm volatile("" : "+s"(ksl), "+s"(cs));
  *(volatile LAS unsigned*)(L + L_ZERO + min(threadIdx.x, 79u) * 4) = 0u;
  unsigned long long tstart64;
  asm volatile("s_memrealtime %0\n\ts_waitcnt lgkmcnt(0)\n\ts_barrier" : "=&s"(tstart64)
               : "s"(x), "s"(kc), "s"(vc), "s"(wq), "s"(wo), "s"(wg), "s"(wu), "s"(wd), "s"(out), "s"(ws) : "memory");
  const unsigned tstart = (unsigned)tstart64;
  const int wave = __builtin_amdgcn_readfirstlane(threadIdx.x >> 6);
#define LANE_IDS int lane_ = threadIdx.x & 63; asm volatile("" : "+v"(lane_)); const int lane = lane_, g = lane >> 4, n = lane & 15;
  const __amdgpu_buffer_rsrc_t rws = mkrsrc(ws, WS_END);
  if (wave == 3) {
    const v4u dx = {(unsigned)(uintptr_t)x, (unsigned)((uintptr_t)x >> 32), 4u * DM * 2u, 0x00020000u};
    const v4u de = {(unsigned)(uintptr_t)ws, (unsigned)((uintptr_t)ws >> 32), 4u, 0x00020000u};
    const int xo = (threadIdx.x & 63) * 16, xo4 = xo < 512 ? xo + 4096 : OOB, z = 0;
    unsigned keep;
    asm volatile("s_mov_b32 %0, m0\n\ts_mov_b32 m0, %4\n\ts_nop 0\n\t"
                 "buffer_load_dwordx4 %1, %2, 0 offen lds\n\tbuffer_load_dwordx4 %1, %2, 0 offen offset:1024 lds\n\t"
                 "buffer_load_dwordx4 %1, %2, 0 offen offset:2048 lds\n\tbuffer_load_dwordx4 %1, %2, 0 offen offset:3072 lds\n\t"
                 "s_mov_b32 m0, %5\n\ts_nop 0\n\tbuffer_load_dwordx4 %8, %2, 0 offen lds\n\t"
                 "s_mov_b32 m0, %6\n\ts_nop 0\n\tbuffer_load_dword %7, %3, 0 offen sc1 lds\n\ts_mov_b32 m0, %0"
                 : "=&s"(keep) : "v"(xo), "s"(dx), "s"(de), "i"(L_XS), "i"(L_XS + 4096), "i"(L_XS + 5120), "v"(z), "v"(xo4) : "memory");
  }
  if (wave == 11) {
    const int po = (c >> 3) * 1024 + (threadIdx.x & 63) * 16;
    unsigned keep;
    asm volatile("s_mov_b32 %0, m0\n\ts_getpc_b64 s[92:93]\n\ts_and_b32 s92, s92, 0xffffff00\n\ts_mov_b32 s94, %2\n\ts_mov_b32 s95, 0x20000\n\t"
                 "s_mov_b32 m0, %3\n\ts_nop 0\n\tbuffer_load_dwordx4 %1, s[92:95], 0 offen lds\n\ts_mov_b32 m0, %0"
                 : "=&s"(keep) : "v"(po), "i"(CODEPF), "i"(L_DUM) : "memory", "scc", "s92", "s93", "s94", "s95");
  }

  if (__builtin_expect(wave >= 4, 0)) {
    LANE_IDS
    const int w = wave - 4;
    const size_t kvo = ((size_t)pair * SK + (SK - WIN) + sp * 512 + ((w + pair) & 7) * 64) * 128;
    const __amdgpu_buffer_rsrc_t rk = mkrsrc(kc + kvo, 16384), rv = mkrsrc(vc + kvo, 16384);
    const int kvo0 = (n >> 2) * 4096 + (n >> 1) * 256 + (n & 1) * 64 + g * 16, vvo0 = g * 4096 + n * 16;
    unsigned tbase = tstart + (unsigned)(((((isS ? 72 : 24) - AHEAD) + 2 * w) * TAU) >> 8);
    const unsigned dstep = isS ? 0u : DSTEP;
    v4u K[16], V[16];
    unsigned shifted = 0;
#define SHIFT { const unsigned f_ = __builtin_amdgcn_readfirstlane(FL[F_SH]); if (f_ != shifted) { tbase += f_ - 1024u; shifted = f_; } }
#pragma unroll
    for (int i = 0; i < 16; ++i) {
      if (i == 4 || i == 8 || i == 12) SHIFT
      if ((i & 1) == 0) PACE_A(8 * i);
      K[i] = ld_nt(rk, (kvo0 ^ ((i >> 2) * 4096)) + ((i >> 1) & 1) * 2048 + (i & 1) * 128);
    }
#define V_LOAD(i) { if (((i) & 1) == 0) PACE_A(128 + 8 * (i)); V[i] = ld_nt(rv, (vvo0 ^ (((i) >> 2) * 4096)) + (((i) & 1) * 8 + ((i) >> 1)) * 256); }
    SHIFT
#pragma unroll
    for (int i = 0; i < VSPLIT; ++i) V_LOAD(i)
    __builtin_amdgcn_sched_barrier(0);
    float m0, m1; unsigned pk;
    auto scores = [&]() __attribute__((always_inline)) {
      f4 E0 = f4{0.f, 0.f, 0.f, 0.f}, E1 = f4{0.f, 0.f, 0.f, 0.f};
      const int qa = L_Q + ((n >> 1) & 1) * 256 + (n & 1) * 64 + g * 16;
#pragma unroll
      for (int tp = 0; tp < 4; ++tp) {
        const int ad = (n >> 2) == tp ? qa : L_ZERO;
        const v4u q0 = *(const LAS v4u*)(L + ad), q1 = *(const LAS v4u*)(L + ad + 128);
        E0 = mfma(q0, K[4 * tp], E0); E1 = mfma(q0, K[4 * tp + 2], E1);
        E0 = mfma(q1, K[4 * tp + 1], E0); E1 = mfma(q1, K[4 * tp + 3], E1);
      }
      const float s0 = rbf(rbf(((n & 1) ? E1[1] : E0[0]) + dpp<XOR1>((n & 1) ? E0[1] : E1[0])) * QK_SCALE);
      const float s1 = rbf(rbf(((n & 1) ? E1[3] : E0[2]) + dpp<XOR1>((n & 1) ? E0[3] : E1[2])) * QK_SCALE);
      m0 = xrow_max(row_max(s0)); m1 = xrow_max(row_max(s1));
      const float p0 = ex2((s0 - m0) * LOG2E), p1 = ex2((s1 - m1) * LOG2E);
      const float l0 = xrow_sum(row_sum(p0)), l1 = xrow_sum(row_sum(p1));
      if (lane == 0) {
        *(LAS float*)(L + L_AM + w * 4) = m0; *(LAS float*)(L + L_AM + 32 + w * 4) = m1;
        *(LAS float*)(L + L_AL + w * 4) = l0; *(LAS float*)(L + L_AL + 32 + w * 4) = l1;
      }
      lds_post(F_L);
      pk = cvtpk(p0, p1);
    };
    const bool early = __builtin_amdgcn_readfirstlane(FL[F_Q]) != 0;
    asm volatile("" ::: "memory");
    if (early) scores();
    __builtin_amdgcn_sched_barrier(0);
#pragma unroll
    for (int i = VSPLIT; i < 16; ++i) V_LOAD(i)
    __builtin_amdgcn_sched_barrier(0);
    lds_post(F_ISS);
    if (!early) { lds_wait<1>(FL + F_Q, 1); scores(); }
    lds_wait<1>(FL + F_L, 8);
    float scale; unsigned t1, t3;
    {
      const int h = g >> 1;
      const f4 ma = *(const LAS f4*)(L + L_AM + h * 32), mb = *(const LAS f4*)(L + L_AM + h * 32 + 16);
      const f4 la = *(const LAS f4*)(L + L_AL + h * 32), lb = *(const LAS f4*)(L + L_AL + h * 32 + 16);
      const float Mh = fmaxf(fmaxf(fmaxf(ma[0], ma[1]), fmaxf(ma[2], ma[3])), fmaxf(fmaxf(mb[0], mb[1]), fmaxf(mb[2], mb[3])));
      float Lh = 0.f;
#pragma unroll
      for (int k = 0; k < 4; ++k) Lh = fmaf(la[k], ex2((ma[k] - Mh) * LOG2E), Lh);
#pragma unroll
      for (int k = 0; k < 4; ++k) Lh = fmaf(lb[k], ex2((mb[k] - Mh) * LOG2E), Lh);
      scale = ex2(((h ? m1 : m0) - Mh) * LOG2E) * rcp(Lh);
      const unsigned th = FL[F_TAG] << 16, ml = __float_as_uint((g & 1) ? Lh : Mh);
      t1 = th | (ml & 0xffffu); t3 = th | (ml >> 16);
    }
    int oh = WS_H1 + (pair * 4 + sp) * 1024 + (g >> 1) * 512 + n * 32 + (g & 1) * 16, la_ = L_AD + lane * 16;
    const unsigned hs = (n & 8) ? 0x0302u : 0x0100u, one = (n & 1) ? ((hs << 16) | 0x0c0cu) : (0x0c0c0000u | hs);
    unsigned sel[4];
#pragma unroll
    for (int r = 0; r < 4; ++r) sel[r] = ((n & 7) >> 1) == r ? one : 0x0c0c0c0cu;
    asm volatile("" : "+v"(scale), "+v"(t1), "+v"(t3), "+v"(oh), "+v"(la_));
    __builtin_amdgcn_s_setprio(3);
    __builtin_amdgcn_sched_barrier(0);
    f4 O = f4{0.f, 0.f, 0.f, 0.f};
#define PV_STEP(i) { const unsigned pb = (unsigned)__builtin_amdgcn_mov_dpp((int)pk, 0x150 + (i), 0xf, 0xf, true); \
      v4u a; a[0] = __builtin_amdgcn_perm(pb, pb, sel[0]); a[1] = __builtin_amdgcn_perm(pb, pb, sel[1]); \
      a[2] = __builtin_amdgcn_perm(pb, pb, sel[2]); a[3] = __builtin_amdgcn_perm(pb, pb, sel[3]); O = mfma(a, V[i], O); }
    PV_STEP(0) PV_STEP(1) PV_STEP(2) PV_STEP(3) PV_STEP(4) PV_STEP(5) PV_STEP(6) PV_STEP(7)
    PV_STEP(8) PV_STEP(9) PV_STEP(10) PV_STEP(11) PV_STEP(12) PV_STEP(13) PV_STEP(14) PV_STEP(15)
    *(LAS f4*)(L + la_ + w * 1024) = O * scale;
    if (lds_arrive(F_D) == 7) {
      f4 t[8];
#pragma unroll
      for (int k = 0; k < 8; ++k) t[k] = *(const LAS f4*)(L + la_ + k * 1024);
      const f4 o = ((t[0] + t[1]) + (t[2] + t[3])) + ((t[4] + t[5]) + (t[6] + t[7]));
      st_sc1(v4u{cvtpk(o[0], o[1]), t1, cvtpk(o[2], o[3]), t3}, rws, oh);
      FL[F_PUB] = 1;
    }
    EXIT_WAVE
  }

  if (__builtin_expect(wave == 3, 0)) {
    __builtin_amdgcn_s_setprio(3);
    LANE_IDS
    const v4u dws = {(unsigned)(uintptr_t)ws, (unsigned)((uintptr_t)ws >> 32), (unsigned)WS_END, 0x00020000u};
    const int zero = 0;
    asm volatile("s_waitcnt vmcnt(0)" ::: "memory");
    const unsigned tag = mktag(*(const volatile LAS unsigned*)(L + L_XS + 5120));
    st_sc1(v4u{tstart, tag, tstart, tag}, rws, (c < 8 && lane < 16) ? WS_T0 + lane * 128 + c * 16 : OOB);
    if (isS) {
      float xf[40], s = 0.f;
#pragma unroll
      for (int i = 0; i < 5; ++i) {
        const v4u X = *(const volatile LAS v4u*)(L + ((i == 4 && n >= 8) ? L_ZERO : L_XS + g * (DM * 2) + (n + 16 * i) * 16));
#pragma unroll
        for (int k = 0; k < 4; ++k) { xf[i * 8 + 2 * k] = blo(X[k]); xf[i * 8 + 2 * k + 1] = bhi(X[k]); }
      }
#pragma unroll
      for (int i = 0; i < 40; ++i) s += xf[i];
      const float mean = row_sum(s) * (1.f / DM);
      float v = 0.f;
#pragma unroll
      for (int i = 0; i < 40; ++i) { const float d = (i >= 32 && n >= 8) ? 0.f : xf[i] - mean; v = fmaf(d, d, v); }
      const float var = row_sum(v) * (1.f / DM);
      const float mb = rbf(mean), rden = rcp(rbf(__builtin_amdgcn_sqrtf(rbf(rbf(var) + 1e-5f))));
#pragma unroll
      for (int i = 0; i < 5; ++i) {
        v4u o;
#pragma unroll
        for (int k = 0; k < 4; ++k) o[k] = cvtpk(rbf(xf[i * 8 + 2 * k] - mb) * rden, rbf(xf[i * 8 + 2 * k + 1] - mb) * rden);
        if (i < 4 || n < 8) *(LAS v4u*)(L + L_XN + g * XN_STR + (n + 16 * i) * 16) = o;
      }
    }
    FL[F_TAG] = tag;
    asm volatile("" ::: "memory");
    FL[F_XN] = 1;
    unsigned t0;
    {
      const int offt = WS_T0 + (c & 15) * 128 + (lane & 7) * 16;
      v4u t;
#pragma nounroll
      for (unsigned s = 0; s < GLB_SPIN; ++s) {
        PLOAD(t, offt, zero);
        asm volatile("s_waitcnt vmcnt(0)" : "+v"(t) :: "memory");
        if (__all((t[1] == tag) & (t[3] == tag))) break;
      }
      float mx = (float)(int)(t[0] - tstart), av = mx;
      mx = fmaxf(mx, dpp<ROR4>(mx)); mx = fmaxf(mx, dpp<ROR2>(mx)); mx = fmaxf(mx, dpp<ROR1>(mx));
      av += dpp<ROR4>(av); av += dpp<ROR2>(av); av += dpp<ROR1>(av);
      const int sh = __builtin_amdgcn_readfirstlane((int)fminf(fmaxf(fmaxf(av * 0.125f, mx - (float)CATCH), -200.f), 200.f));
      t0 = tstart + (unsigned)sh;
      FL[F_SH] = (unsigned)(sh + 1024);
    }
    {
      const int off = WS_S1 + (pair >> 4) * S1_ROW + (pair & 15) * 1024 + lane * 16;
      v4u r;
#pragma nounroll
      for (unsigned s = 0; s < GLB_SPIN; ++s) {
        PLOAD(r, off, zero);
        asm volatile("s_waitcnt vmcnt(0)" : "+v"(r) :: "memory");
        if (__all((r[1] == tag) & (r[3] == tag))) break;
      }
      *(LAS v2u*)(L + L_Q + lane * 8) = v2u{r[0], r[2]};
      asm volatile("" ::: "memory");
      FL[F_Q] = 1;
    }
    if (!isS) {
      const int rs = (c - N_S1) / 3, up = FF * 4;
      v4u G[5], U[5];
      int off[5], loff[5];
#pragma unroll
      for (int i = 0; i < 5; ++i) {
        const int t = min(lane + 64 * i, 287), b = t / 72, qd = t - b * 72;
        off[i] = WS_S1 + b * S1_ROW + (NQ + 288 * rs + 4 * qd) * 4; loff[i] = b * H_STR + qd * 8;
      }
#pragma nounroll
      for (unsigned s = 0; s < GLB_SPIN; ++s) {
#pragma unroll
        for (int i = 0; i < 5; ++i) { PLOAD(G[i], off[i], zero); PLOAD(U[i], off[i], up); }
        asm volatile("s_waitcnt vmcnt(0)" : "+v"(G[0]), "+v"(G[1]), "+v"(G[2]), "+v"(G[3]), "+v"(G[4]),
                     "+v"(U[0]), "+v"(U[1]), "+v"(U[2]), "+v"(U[3]), "+v"(U[4]) :: "memory");
        bool ok = true;
#pragma unroll
        for (int i = 0; i < 5; ++i) ok &= (G[i][1] == tag) & (G[i][3] == tag) & (U[i][1] == tag) & (U[i][3] == tag);
        if (__all(ok)) break;
        __builtin_amdgcn_s_sleep(4);
      }
      if (lane < 32) *(LAS v2u*)(L + L_H + (lane >> 3) * H_STR + 576 + (lane & 7) * 8) = v2u{0u, 0u};
#pragma unroll
      for (int i = 0; i < 5; ++i) {
        float hv[4];
#pragma unroll
        for (int k = 0; k < 4; ++k) {
          const unsigned gw = G[i][(k >> 1) * 2], uw = U[i][(k >> 1) * 2];
          const float a = (k & 1) ? bhi(gw) : blo(gw), u = (k & 1) ? bhi(uw) : blo(uw);
          const float z2 = (-2.f * 0.7978845608028654f * LOG2E) * (a + 0.044715f * a * a * a);
          hv[k] = rbf(a * rcp(1.f + ex2(z2))) * u;
        }
        *(LAS v2u*)(L + L_H + loff[i]) = v2u{cvtpk(hv[0], hv[1]), cvtpk(hv[2], hv[3])};
      }
      asm volatile("" ::: "memory");
      FL[F_H] = 1;
    }
    if (c >= N_WO) EXIT_WAVE
    const int fcs = c / 48, fr = c - fcs * 48, fw = fr >> 4, fpc = (fr >> 3) & 1, fgr = fr & 7;
    int oo = ((2 * (fgr >> 2) + fpc) * DM + 64 * (3 * fcs + fw) + 16 * (fgr & 3) + 2 * (lane & 7) + (g & 1)) * 2;
    oo = (c < N_FIN && (lane & 40) == 0) ? oo : OOB;
    float xres = blo(__builtin_amdgcn_raw_buffer_load_b16(mkrsrc(x, 4 * DM * 2), oo, 0, 0));
    asm volatile("" : "+v"(xres), "+v"(oo));
    {
      const int hh = ksl >> 1, pc = (((n & 3) * 16 + (hh >> 1)) * 4 + 2 * (n >> 3));
      const int o1 = WS_H1 + pc * 1024 + (hh & 1) * 512 + (8 * (ksl & 1) + 4 * ((n >> 2) & 1) + g) * 32;
      lds_wait<1>(FL + F_PUB, 1);
      wait_until(max((int)((unsigned)__builtin_amdgcn_s_memrealtime() - t0) + D_AB, T_A), t0);
      asm volatile(
        "buffer_load_dwordx4 v[120:123], %[o1], %[d], 0 offen offset:0 sc1\n\t"
        "buffer_load_dwordx4 v[124:127], %[o1], %[d], 0 offen offset:16 sc1\n\t"
        "buffer_load_dwordx4 v[128:131], %[o1], %[d], 0 offen offset:1024 sc1\n\t"
        "buffer_load_dwordx4 v[132:135], %[o1], %[d], 0 offen offset:1040 sc1\n\t"
        "s_sleep " STR(STAG) "\n\t"
        "buffer_load_dwordx4 v[136:139], %[o1], %[d], 0 offen offset:0 sc1\n\t"
        "buffer_load_dwordx4 v[140:143], %[o1], %[d], 0 offen offset:16 sc1\n\t"
        "buffer_load_dwordx4 v[144:147], %[o1], %[d], 0 offen offset:1024 sc1\n\t"
        "buffer_load_dwordx4 v[148:151], %[o1], %[d], 0 offen offset:1040 sc1\n\t"
        "s_waitcnt vmcnt(4)\n\t"
        "v_bitop3_b32 v112, v121, v123, %[t] bitop3:0x7e\n\t"
        "v_bitop3_b32 v113, v125, v127, %[t] bitop3:0x7e\n\t"
        "v_bitop3_b32 v114, v129, v131, %[t] bitop3:0x7e\n\t"
        "v_bitop3_b32 v115, v133, v135, %[t] bitop3:0x7e\n\t"
        "v_or3_b32 v112, v112, v113, v114\n\t"
        "v_or_b32 v112, v112, v115\n\t"
        "v_cmp_gt_u32 vcc, 0x10000, v112\n\t"
        "v_perm_b32 v108, v123, v121, %[sl]\n\t"
        "v_perm_b32 v109, v127, v125, %[sl]\n\t"
        "v_perm_b32 v110, v131, v129, %[sl]\n\t"
        "v_perm_b32 v111, v135, v133, %[sl]\n\t"
        "s_cmp_eq_u64 vcc, exec\n\t"
        "s_cbranch_scc0 2f\n\t"
        "v_max_f32 v112, v108, v110\n\t"
        "v_lshlrev_b32 v80, 16, v120\n\t"
        "v_and_b32 v81, 0xffff0000, v120\n\t"
        "v_max_f32_dpp v112, v112, v112 row_ror:8 row_mask:0xf bank_mask:0xf\n\t"
        "v_lshlrev_b32 v82, 16, v122\n\t"
        "v_sub_f32 v113, v108, v112\n\t"
        "v_sub_f32 v114, v110, v112\n\t"
        "v_mul_f32 v113, 0x3fb8aa3b, v113\n\t"
        "v_mul_f32 v114, 0x3fb8aa3b, v114\n\t"
        "v_exp_f32 v113, v113\n\t"
        "v_exp_f32 v114, v114\n\t"
        "v_and_b32 v83, 0xffff0000, v122\n\t"
        "v_lshlrev_b32 v84, 16, v124\n\t"
        "v_and_b32 v85, 0xffff0000, v124\n\t"
        "v_mul_f32 v116, v109, v113\n\t"
        "v_mul_f32 v118, v111, v114\n\t"
        "v_add_f32 v112, v116, v118\n\t"
        "v_lshlrev_b32 v86, 16, v126\n\t"
        "v_and_b32 v87, 0xffff0000, v126\n\t"
        "v_add_f32_dpp v112, v112, v112 row_ror:8 row_mask:0xf bank_mask:0xf\n\t"
        "v_lshlrev_b32 v88, 16, v128\n\t"
        "v_rcp_f32 v114, v112\n\t"
        "v_and_b32 v89, 0xffff0000, v128\n\t"
        "v_lshlrev_b32 v90, 16, v130\n\t"
        "v_and_b32 v91, 0xffff0000, v130\n\t"
        "v_lshlrev_b32 v92, 16, v132\n\t"
        "v_and_b32 v93, 0xffff0000, v132\n\t"
        "v_lshlrev_b32 v94, 16, v134\n\t"
        "v_and_b32 v95, 0xffff0000, v134\n\t"
        "1:\n\t"
        "v_pk_mul_f32 v[88:89], v[88:89], v[118:119] op_sel_hi:[1,0]\n\t"
        "v_pk_mul_f32 v[90:91], v[90:91], v[118:119] op_sel_hi:[1,0]\n\t"
        "v_pk_mul_f32 v[92:93], v[92:93], v[118:119] op_sel_hi:[1,0]\n\t"
        "v_pk_mul_f32 v[94:95], v[94:95], v[118:119] op_sel_hi:[1,0]\n\t"
        "v_pk_fma_f32 v[80:81], v[80:81], v[116:117], v[88:89] op_sel_hi:[1,0,1]\n\t"
        "v_pk_fma_f32 v[82:83], v[82:83], v[116:117], v[90:91] op_sel_hi:[1,0,1]\n\t"
        "v_pk_fma_f32 v[84:85], v[84:85], v[116:117], v[92:93] op_sel_hi:[1,0,1]\n\t"
        "v_pk_fma_f32 v[86:87], v[86:87], v[116:117], v[94:95] op_sel_hi:[1,0,1]\n\t"
        "v_add_f32_dpp v80, v80, v80 row_ror:8 row_mask:0xf bank_mask:0xf\n\t"
        "v_add_f32_dpp v81, v81, v81 row_ror:8 row_mask:0xf bank_mask:0xf\n\t"
        "v_add_f32_dpp v82, v82, v82 row_ror:8 row_mask:0xf bank_mask:0xf\n\t"
        "v_add_f32_dpp v83, v83, v83 row_ror:8 row_mask:0xf bank_mask:0xf\n\t"
        "v_add_f32_dpp v84, v84, v84 row_ror:8 row_mask:0xf bank_mask:0xf\n\t"
        "v_add_f32_dpp v85, v85, v85 row_ror:8 row_mask:0xf bank_mask:0xf\n\t"
        "v_add_f32_dpp v86, v86, v86 row_ror:8 row_mask:0xf bank_mask:0xf\n\t"
        "v_add_f32_dpp v87, v87, v87 row_ror:8 row_mask:0xf bank_mask:0xf\n\t"
        "v_pk_mul_f32 v[80:81], v[80:81], v[114:115] op_sel_hi:[1,0]\n\t"
        "v_pk_mul_f32 v[82:83], v[82:83], v[114:115] op_sel_hi:[1,0]\n\t"
        "v_pk_mul_f32 v[84:85], v[84:85], v[114:115] op_sel_hi:[1,0]\n\t"
        "v_pk_mul_f32 v[86:87], v[86:87], v[114:115] op_sel_hi:[1,0]\n\t"
        "v_cvt_pk_bf16_f32 v88, v80, v81\n\t"
        "v_cvt_pk_bf16_f32 v89, v82, v83\n\t"
        "v_cvt_pk_bf16_f32 v90, v84, v85\n\t"
        "v_cvt_pk_bf16_f32 v91, v86, v87\n\t"
        "ds_write_b128 %[la], v[88:91]\n\t"
        "ds_write_b32 %[lf], %[one]\n\t"
        "s_branch 5f\n\t"
        "2:\n\t"
        "s_mov_b64 s[70:71], vcc\n\t"
        "s_mov_b32 s72, " STR(GLB_N) "\n\t"
        "v_mov_b32 v100, v120\n\t"
        "v_mov_b32 v101, v122\n\t"
        "v_mov_b32 v102, v124\n\t"
        "v_mov_b32 v103, v126\n\t"
        "v_mov_b32 v104, v128\n\t"
        "v_mov_b32 v105, v130\n\t"
        "v_mov_b32 v106, v132\n\t"
        "v_mov_b32 v107, v134\n\t"
        "s_branch 7f\n\t"
        "3:\n\t"
        "v_cndmask_b32_e64 v119, %[o1], 2.0, s[70:71]\n\t"
        "buffer_load_dwordx4 v[136:139], v119, %[d], 0 offen offset:0 sc1\n\t"
        "buffer_load_dwordx4 v[140:143], v119, %[d], 0 offen offset:16 sc1\n\t"
        "buffer_load_dwordx4 v[144:147], v119, %[d], 0 offen offset:1024 sc1\n\t"
        "buffer_load_dwordx4 v[148:151], v119, %[d], 0 offen offset:1040 sc1\n\t"
        "s_waitcnt vmcnt(4)\n\t"
        "v_bitop3_b32 v112, v121, v123, %[t] bitop3:0x7e\n\t"
        "v_bitop3_b32 v113, v125, v127, %[t] bitop3:0x7e\n\t"
        "v_bitop3_b32 v114, v129, v131, %[t] bitop3:0x7e\n\t"
        "v_bitop3_b32 v115, v133, v135, %[t] bitop3:0x7e\n\t"
        "v_or3_b32 v112, v112, v113, v114\n\t"
        "v_or_b32 v112, v112, v115\n\t"
        "v_cmp_gt_u32 vcc, 0x10000, v112\n\t"
        "v_perm_b32 v113, v123, v121, %[sl]\n\t"
        "v_perm_b32 v114, v127, v125, %[sl]\n\t"
        "v_perm_b32 v115, v131, v129, %[sl]\n\t"
        "v_perm_b32 v116, v135, v133, %[sl]\n\t"
        "s_or_b64 s[70:71], s[70:71], vcc\n\t"
        "v_cndmask_b32 v100, v100, v120, vcc\n\t"
        "v_cndmask_b32 v101, v101, v122, vcc\n\t"
        "v_cndmask_b32 v102, v102, v124, vcc\n\t"
        "v_cndmask_b32 v103, v103, v126, vcc\n\t"
        "v_cndmask_b32 v104, v104, v128, vcc\n\t"
        "v_cndmask_b32 v105, v105, v130, vcc\n\t"
        "v_cndmask_b32 v106, v106, v132, vcc\n\t"
        "v_cndmask_b32 v107, v107, v134, vcc\n\t"
        "v_cndmask_b32 v108, v108, v113, vcc\n\t"
        "v_cndmask_b32 v109, v109, v114, vcc\n\t"
        "v_cndmask_b32 v110, v110, v115, vcc\n\t"
        "v_cndmask_b32 v111, v111, v116, vcc\n\t"
        "s_cmp_eq_u64 s[70:71], exec\n\t"
        "s_cbranch_scc1 4f\n\t"
        "7:\n\t"
        "v_cndmask_b32_e64 v119, %[o1], 2.0, s[70:71]\n\t"
        "buffer_load_dwordx4 v[120:123], v119, %[d], 0 offen offset:0 sc1\n\t"
        "buffer_load_dwordx4 v[124:127], v119, %[d], 0 offen offset:16 sc1\n\t"
        "buffer_load_dwordx4 v[128:131], v119, %[d], 0 offen offset:1024 sc1\n\t"
        "buffer_load_dwordx4 v[132:135], v119, %[d], 0 offen offset:1040 sc1\n\t"
        "s_waitcnt vmcnt(4)\n\t"
        "v_bitop3_b32 v112, v137, v139, %[t] bitop3:0x7e\n\t"
        "v_bitop3_b32 v113, v141, v143, %[t] bitop3:0x7e\n\t"
        "v_bitop3_b32 v114, v145, v147, %[t] bitop3:0x7e\n\t"
        "v_bitop3_b32 v115, v149, v151, %[t] bitop3:0x7e\n\t"
        "v_or3_b32 v112, v112, v113, v114\n\t"
        "v_or_b32 v112, v112, v115\n\t"
        "v_cmp_gt_u32 vcc, 0x10000, v112\n\t"
        "v_perm_b32 v113, v139, v137, %[sl]\n\t"
        "v_perm_b32 v114, v143, v141, %[sl]\n\t"
        "v_perm_b32 v115, v147, v145, %[sl]\n\t"
        "v_perm_b32 v116, v151, v149, %[sl]\n\t"
        "s_or_b64 s[70:71], s[70:71], vcc\n\t"
        "v_cndmask_b32 v100, v100, v136, vcc\n\t"
        "v_cndmask_b32 v101, v101, v138, vcc\n\t"
        "v_cndmask_b32 v102, v102, v140, vcc\n\t"
        "v_cndmask_b32 v103, v103, v142, vcc\n\t"
        "v_cndmask_b32 v104, v104, v144, vcc\n\t"
        "v_cndmask_b32 v105, v105, v146, vcc\n\t"
        "v_cndmask_b32 v106, v106, v148, vcc\n\t"
        "v_cndmask_b32 v107, v107, v150, vcc\n\t"
        "v_cndmask_b32 v108, v108, v113, vcc\n\t"
        "v_cndmask_b32 v109, v109, v114, vcc\n\t"
        "v_cndmask_b32 v110, v110, v115, vcc\n\t"
        "v_cndmask_b32 v111, v111, v116, vcc\n\t"
        "s_cmp_eq_u64 s[70:71], exec\n\t"
        "s_cbranch_scc1 4f\n\t"
        "s_bcnt0_i32_b64 s73, s[70:71]\n\t"
        "s_cmp_lt_u32 s73, 17\n\t"
        "s_cbranch_scc1 6f\n\t"
        "s_sleep 12\n\t"
        "6:\n\t"
        "s_sub_u32 s72, s72, 1\n\t"
        "s_cbranch_scc0 3b\n\t"
        "4:\n\t"
        "v_max_f32 v112, v108, v110\n\t"
        "v_lshlrev_b32 v80, 16, v100\n\t"
        "v_and_b32 v81, 0xffff0000, v100\n\t"
        "v_max_f32_dpp v112, v112, v112 row_ror:8 row_mask:0xf bank_mask:0xf\n\t"
        "v_lshlrev_b32 v82, 16, v101\n\t"
        "v_sub_f32 v113, v108, v112\n\t"
        "v_sub_f32 v114, v110, v112\n\t"
        "v_mul_f32 v113, 0x3fb8aa3b, v113\n\t"
        "v_mul_f32 v114, 0x3fb8aa3b, v114\n\t"
        "v_exp_f32 v113, v113\n\t"
        "v_exp_f32 v114, v114\n\t"
        "v_and_b32 v83, 0xffff0000, v101\n\t"
        "v_lshlrev_b32 v84, 16, v102\n\t"
        "v_and_b32 v85, 0xffff0000, v102\n\t"
        "v_mul_f32 v116, v109, v113\n\t"
        "v_mul_f32 v118, v111, v114\n\t"
        "v_add_f32 v112, v116, v118\n\t"
        "v_lshlrev_b32 v86, 16, v103\n\t"
        "v_and_b32 v87, 0xffff0000, v103\n\t"
        "v_add_f32_dpp v112, v112, v112 row_ror:8 row_mask:0xf bank_mask:0xf\n\t"
        "v_lshlrev_b32 v88, 16, v104\n\t"
        "v_rcp_f32 v114, v112\n\t"
        "v_and_b32 v89, 0xffff0000, v104\n\t"
        "v_lshlrev_b32 v90, 16, v105\n\t"
        "v_and_b32 v91, 0xffff0000, v105\n\t"
        "v_lshlrev_b32 v92, 16, v106\n\t"
        "v_and_b32 v93, 0xffff0000, v106\n\t"
        "v_lshlrev_b32 v94, 16, v107\n\t"
        "v_and_b32 v95, 0xffff0000, v107\n\t"
        "s_branch 1b\n\t"
        "5:\n\t"
        "s_waitcnt vmcnt(0)"
        :: [o1] "v"(o1), [d] "s"(dws), [t] "v"(tag << 16), [sl] "s"(0x05040100u), [la] "v"(L_AF + lane * 16), [lf] "v"(L_FLAG + 4 * F_A), [one] "v"(1u)
        : "memory", "vcc", "scc", "s70", "s71", "s72", "s73", "v80", "v81", "v82", "v83", "v84", "v85", "v86", "v87", "v88", "v89", "v90", "v91", "v92", "v93", "v94", "v95", "v96", "v97", "v98", "v99", "v100", "v101", "v102", "v103", "v104", "v105", "v106", "v107", "v108", "v109", "v110", "v111", "v112", "v113", "v114", "v115", "v116", "v117", "v118", "v119", "v120", "v121", "v122", "v123", "v124", "v125", "v126", "v127", "v128", "v129", "v130", "v131", "v132", "v133", "v134", "v135", "v136", "v137", "v138", "v139", "v140", "v141", "v142", "v143", "v144", "v145", "v146", "v147", "v148", "v149", "v150", "v151");
    }
    if (c >= N_FIN) EXIT_WAVE
    const int lo_ = fw * 2048 + fpc * 1024 + fgr * 128 + (lane & 7) * 16;
    int o[9];
    o[8] = WS_F2 + (fcs * 16 + (lane >> 3)) * 6144 + lo_;
#pragma unroll
    for (int i = 0; i < 8; ++i) { o[i] = WS_H2 + (fcs * 64 + 8 * i + (lane >> 3)) * 6144 + lo_; }
    const v4u dout = {(unsigned)(uintptr_t)out, (unsigned)((uintptr_t)out >> 32), 4u * DM * 2u, 0x00020000u};
    int oe = (c | lane) ? OOB : WS_EPOCH;
    asm volatile("" : "+v"(oe));
    lds_wait<0>(FL + F_H2, 3);
    wait_until(max((int)((unsigned)__builtin_amdgcn_s_memrealtime() - t0) + D_AB, T_B), t0);
    asm volatile(
        "s_mov_b32 s73, 0xc000\n\t"
        "buffer_load_dwordx4 v[88:91], %[o0], %[d], 0 offen sc1\n\t"
        "buffer_load_dwordx4 v[92:95], %[o1], %[d], 0 offen sc1\n\t"
        "buffer_load_dwordx4 v[96:99], %[o2], %[d], 0 offen sc1\n\t"
        "buffer_load_dwordx4 v[100:103], %[o3], %[d], 0 offen sc1\n\t"
        "buffer_load_dwordx4 v[104:107], %[o4], %[d], 0 offen sc1\n\t"
        "buffer_load_dwordx4 v[108:111], %[o5], %[d], 0 offen sc1\n\t"
        "buffer_load_dwordx4 v[112:115], %[o6], %[d], 0 offen sc1\n\t"
        "buffer_load_dwordx4 v[116:119], %[o7], %[d], 0 offen sc1\n\t"
        "buffer_load_dwordx4 v[120:123], %[o8], %[d], 0 offen sc1\n\t"
        "buffer_load_dwordx4 v[124:127], %[o8], %[d], s73 offen sc1\n\t"
        "s_sleep " STR(STAG) "\n\t"
        "buffer_load_dwordx4 v[128:131], %[o0], %[d], 0 offen sc1\n\t"
        "buffer_load_dwordx4 v[132:135], %[o1], %[d], 0 offen sc1\n\t"
        "buffer_load_dwordx4 v[136:139], %[o2], %[d], 0 offen sc1\n\t"
        "buffer_load_dwordx4 v[140:143], %[o3], %[d], 0 offen sc1\n\t"
        "buffer_load_dwordx4 v[144:147], %[o4], %[d], 0 offen sc1\n\t"
        "buffer_load_dwordx4 v[148:151], %[o5], %[d], 0 offen sc1\n\t"
        "buffer_load_dwordx4 v[152:155], %[o6], %[d], 0 offen sc1\n\t"
        "buffer_load_dwordx4 v[156:159], %[o7], %[d], 0 offen sc1\n\t"
        "buffer_load_dwordx4 v[160:163], %[o8], %[d], 0 offen sc1\n\t"
        "buffer_load_dwordx4 v[164:167], %[o8], %[d], s73 offen sc1\n\t"
        "s_waitcnt vmcnt(10)\n\t"
        "v_bitop3_b32 v78, v89, v91, %[t] bitop3:0x7e\n\t"
        "v_bitop3_b32 v79, v93, v95, %[t] bitop3:0x7e\n\t"
        "v_bitop3_b32 v80, v97, v99, %[t] bitop3:0x7e\n\t"
        "v_bitop3_b32 v81, v101, v103, %[t] bitop3:0x7e\n\t"
        "v_bitop3_b32 v82, v105, v107, %[t] bitop3:0x7e\n\t"
        "v_bitop3_b32 v83, v109, v111, %[t] bitop3:0x7e\n\t"
        "v_bitop3_b32 v84, v113, v115, %[t] bitop3:0x7e\n\t"
        "v_bitop3_b32 v85, v117, v119, %[t] bitop3:0x7e\n\t"
        "v_bitop3_b32 v86, v121, v123, %[t] bitop3:0x7e\n\t"
        "v_bitop3_b32 v87, v125, v127, %[t] bitop3:0x7e\n\t"
        "v_or3_b32 v52, v78, v79, v80\n\t"
        "v_or3_b32 v53, v81, v82, v83\n\t"
        "v_or3_b32 v54, v84, v85, v86\n\t"
        "v_or3_b32 v52, v52, v53, v87\n\t"
        "v_or_b32 v52, v52, v54\n\t"
        "v_cmp_eq_u32 vcc, 0, v52\n\t"
        "v_add_f32 v76, v120, v124\n\t"
        "v_add_f32 v77, v122, v126\n\t"
        "v_add_f32 v68, v88, v92\n\t"
        "v_add_f32 v69, v96, v100\n\t"
        "v_add_f32 v70, v104, v108\n\t"
        "v_add_f32 v71, v112, v116\n\t"
        "v_add_f32 v72, v90, v94\n\t"
        "v_add_f32 v73, v98, v102\n\t"
        "v_add_f32 v74, v106, v110\n\t"
        "v_add_f32 v75, v114, v118\n\t"
        "s_cmp_eq_u64 vcc, exec\n\t"
        "s_cbranch_scc0 2f\n\t"
        "1:\n\t"
        "v_add_f32_dpp v76, v76, v76 row_ror:8 row_mask:0xf bank_mask:0xf\n\t"
        "v_add_f32_dpp v77, v77, v77 row_ror:8 row_mask:0xf bank_mask:0xf\n\t"
        "v_add_f32 v68, v68, v69\n\t"
        "v_add_f32 v70, v70, v71\n\t"
        "v_permlane16_swap_b32 v76, v77\n\t"
        "v_add_f32 v72, v72, v73\n\t"
        "v_add_f32 v74, v74, v75\n\t"
        "v_add_f32 v76, v76, v77\n\t"
        "v_add_f32 v68, v68, v70\n\t"
        "v_mov_b32 v77, v76\n\t"
        "v_add_f32 v72, v72, v74\n\t"
        "v_add_f32_dpp v68, v68, v68 row_ror:8 row_mask:0xf bank_mask:0xf\n\t"
        "v_permlane32_swap_b32 v76, v77\n\t"
        "v_add_f32_dpp v72, v72, v72 row_ror:8 row_mask:0xf bank_mask:0xf\n\t"
        "v_add_f32 v76, v76, v77\n\t"
        "v_cvt_pk_bf16_f32 v76, v76, v76\n\t"
        "v_permlane16_swap_b32 v68, v72\n\t"
        "v_lshlrev_b32 v76, 16, v76\n\t"
        "v_add_f32 v68, v68, v72\n\t"
        "v_mul_f32 v77, 0xbfb8aa3b, v76\n\t"
        "v_mov_b32 v72, v68\n\t"
        "v_exp_f32 v77, v77\n\t"
        "s_nop 0\n\t"
        "v_permlane32_swap_b32 v68, v72\n\t"
        "v_add_f32 v77, 1.0, v77\n\t"
        "v_add_f32 v68, v68, v72\n\t"
        "v_rcp_f32 v77, v77\n\t"
        "v_cvt_pk_bf16_f32 v68, v68, v68\n\t"
        "v_mul_f32 v76, v76, v77\n\t"
        "v_lshlrev_b32 v68, 16, v68\n\t"
        "v_cvt_pk_bf16_f32 v76, v76, v76\n\t"
        "v_lshlrev_b32 v76, 16, v76\n\t"
        "v_mul_f32 v68, v68, v76\n\t"
        "v_cvt_pk_bf16_f32 v68, v68, v68\n\t"
        "v_lshlrev_b32 v68, 16, v68\n\t"
        "v_add_f32 v68, %[x], v68\n\t"
        "v_cvt_pk_bf16_f32 v68, v68, v68\n\t"
        "buffer_store_short v68, %[oo], %[ro], 0 offen sc0 sc1\n\t"
        "buffer_store_dword %[t], %[oe], %[d], 0 offen sc1\n\t"
        "s_branch 5f\n\t"
        "2:\n\t"
        "s_mov_b32 s72, " STR(GLB_N) "\n\t"
        "v_or_b32 v86, v86, v87\n\t"
        "v_cmp_eq_u32 s[74:75], 0, v78\n\t"
        "v_cmp_eq_u32 s[76:77], 0, v79\n\t"
        "v_cmp_eq_u32 s[78:79], 0, v80\n\t"
        "v_cmp_eq_u32 s[80:81], 0, v81\n\t"
        "v_cmp_eq_u32 s[82:83], 0, v82\n\t"
        "v_cmp_eq_u32 s[84:85], 0, v83\n\t"
        "v_cmp_eq_u32 s[86:87], 0, v84\n\t"
        "v_cmp_eq_u32 s[88:89], 0, v85\n\t"
        "v_cmp_eq_u32 s[90:91], 0, v86\n\t"
        "v_cndmask_b32_e64 %[o0], %[o0], 2.0, s[74:75]\n\t"
        "v_cndmask_b32_e64 %[o1], %[o1], 2.0, s[76:77]\n\t"
        "v_cndmask_b32_e64 %[o2], %[o2], 2.0, s[78:79]\n\t"
        "v_cndmask_b32_e64 %[o3], %[o3], 2.0, s[80:81]\n\t"
        "v_cndmask_b32_e64 %[o4], %[o4], 2.0, s[82:83]\n\t"
        "v_cndmask_b32_e64 %[o5], %[o5], 2.0, s[84:85]\n\t"
        "v_cndmask_b32_e64 %[o6], %[o6], 2.0, s[86:87]\n\t"
        "v_cndmask_b32_e64 %[o7], %[o7], 2.0, s[88:89]\n\t"
        "v_cndmask_b32_e64 %[o8], %[o8], 2.0, s[90:91]\n\t"
        "v_min3_u32 v78, %[o0], %[o1], %[o2]\n\t"
        "v_min3_u32 v79, %[o3], %[o4], %[o5]\n\t"
        "v_min3_u32 v80, %[o6], %[o7], %[o8]\n\t"
        "v_min3_u32 v78, v78, v79, v80\n\t"
        "v_cmp_eq_u32 vcc, 2.0, v78\n\t"
        "v_add_f32 v79, v120, v124\n\t"
        "v_add_f32 v80, v122, v126\n\t"
        "v_cndmask_b32_e64 v52, v52, v88, s[74:75]\n\t"
        "v_cndmask_b32_e64 v60, v60, v90, s[74:75]\n\t"
        "v_cndmask_b32_e64 v53, v53, v92, s[76:77]\n\t"
        "v_cndmask_b32_e64 v61, v61, v94, s[76:77]\n\t"
        "v_cndmask_b32_e64 v54, v54, v96, s[78:79]\n\t"
        "v_cndmask_b32_e64 v62, v62, v98, s[78:79]\n\t"
        "v_cndmask_b32_e64 v55, v55, v100, s[80:81]\n\t"
        "v_cndmask_b32_e64 v63, v63, v102, s[80:81]\n\t"
        "v_cndmask_b32_e64 v56, v56, v104, s[82:83]\n\t"
        "v_cndmask_b32_e64 v64, v64, v106, s[82:83]\n\t"
        "v_cndmask_b32_e64 v57, v57, v108, s[84:85]\n\t"
        "v_cndmask_b32_e64 v65, v65, v110, s[84:85]\n\t"
        "v_cndmask_b32_e64 v58, v58, v112, s[86:87]\n\t"
        "v_cndmask_b32_e64 v66, v66, v114, s[86:87]\n\t"
        "v_cndmask_b32_e64 v59, v59, v116, s[88:89]\n\t"
        "v_cndmask_b32_e64 v67, v67, v118, s[88:89]\n\t"
        "v_cndmask_b32_e64 v76, v76, v79, s[90:91]\n\t"
        "v_cndmask_b32_e64 v77, v77, v80, s[90:91]\n\t"
        "s_branch 7f\n\t"
        "3:\n\t"
        "buffer_load_dwordx4 v[128:131], %[o0], %[d], 0 offen sc1\n\t"
        "buffer_load_dwordx4 v[132:135], %[o1], %[d], 0 offen sc1\n\t"
        "buffer_load_dwordx4 v[136:139], %[o2], %[d], 0 offen sc1\n\t"
        "buffer_load_dwordx4 v[140:143], %[o3], %[d], 0 offen sc1\n\t"
        "buffer_load_dwordx4 v[144:147], %[o4], %[d], 0 offen sc1\n\t"
        "buffer_load_dwordx4 v[148:151], %[o5], %[d], 0 offen sc1\n\t"
        "buffer_load_dwordx4 v[152:155], %[o6], %[d], 0 offen sc1\n\t"
        "buffer_load_dwordx4 v[156:159], %[o7], %[d], 0 offen sc1\n\t"
        "buffer_load_dwordx4 v[160:163], %[o8], %[d], 0 offen sc1\n\t"
        "buffer_load_dwordx4 v[164:167], %[o8], %[d], s73 offen sc1\n\t"
        "s_waitcnt vmcnt(10)\n\t"
        "v_bitop3_b32 v78, v89, v91, %[t] bitop3:0x7e\n\t"
        "v_bitop3_b32 v79, v93, v95, %[t] bitop3:0x7e\n\t"
        "v_bitop3_b32 v80, v97, v99, %[t] bitop3:0x7e\n\t"
        "v_bitop3_b32 v81, v101, v103, %[t] bitop3:0x7e\n\t"
        "v_bitop3_b32 v82, v105, v107, %[t] bitop3:0x7e\n\t"
        "v_bitop3_b32 v83, v109, v111, %[t] bitop3:0x7e\n\t"
        "v_bitop3_b32 v84, v113, v115, %[t] bitop3:0x7e\n\t"
        "v_bitop3_b32 v85, v117, v119, %[t] bitop3:0x7e\n\t"
        "v_bitop3_b32 v86, v121, v123, %[t] bitop3:0x7e\n\t"
        "v_bitop3_b32 v87, v125, v127, %[t] bitop3:0x7e\n\t"
        "v_or_b32 v86, v86, v87\n\t"
        "v_cmp_eq_u32 s[74:75], 0, v78\n\t"
        "v_cmp_eq_u32 s[76:77], 0, v79\n\t"
        "v_cmp_eq_u32 s[78:79], 0, v80\n\t"
        "v_cmp_eq_u32 s[80:81], 0, v81\n\t"
        "v_cmp_eq_u32 s[82:83], 0, v82\n\t"
        "v_cmp_eq_u32 s[84:85], 0, v83\n\t"
        "v_cmp_eq_u32 s[86:87], 0, v84\n\t"
        "v_cmp_eq_u32 s[88:89], 0, v85\n\t"
        "v_cmp_eq_u32 s[90:91], 0, v86\n\t"
        "v_cndmask_b32_e64 %[o0], %[o0], 2.0, s[74:75]\n\t"
        "v_cndmask_b32_e64 %[o1], %[o1], 2.0, s[76:77]\n\t"
        "v_cndmask_b32_e64 %[o2], %[o2], 2.0, s[78:79]\n\t"
        "v_cndmask_b32_e64 %[o3], %[o3], 2.0, s[80:81]\n\t"
        "v_cndmask_b32_e64 %[o4], %[o4], 2.0, s[82:83]\n\t"
        "v_cndmask_b32_e64 %[o5], %[o5], 2.0, s[84:85]\n\t"
        "v_cndmask_b32_e64 %[o6], %[o6], 2.0, s[86:87]\n\t"
        "v_cndmask_b32_e64 %[o7], %[o7], 2.0, s[88:89]\n\t"
        "v_cndmask_b32_e64 %[o8], %[o8], 2.0, s[90:91]\n\t"
        "v_min3_u32 v78, %[o0], %[o1], %[o2]\n\t"
        "v_min3_u32 v79, %[o3], %[o4], %[o5]\n\t"
        "v_min3_u32 v80, %[o6], %[o7], %[o8]\n\t"
        "v_min3_u32 v78, v78, v79, v80\n\t"
        "v_cmp_eq_u32 vcc, 2.0, v78\n\t"
        "v_add_f32 v79, v120, v124\n\t"
        "v_add_f32 v80, v122, v126\n\t"
        "v_cndmask_b32_e64 v52, v52, v88, s[74:75]\n\t"
        "v_cndmask_b32_e64 v60, v60, v90, s[74:75]\n\t"
        "v_cndmask_b32_e64 v53, v53, v92, s[76:77]\n\t"
        "v_cndmask_b32_e64 v61, v61, v94, s[76:77]\n\t"
        "v_cndmask_b32_e64 v54, v54, v96, s[78:79]\n\t"
        "v_cndmask_b32_e64 v62, v62, v98, s[78:79]\n\t"
        "v_cndmask_b32_e64 v55, v55, v100, s[80:81]\n\t"
        "v_cndmask_b32_e64 v63, v63, v102, s[80:81]\n\t"
        "v_cndmask_b32_e64 v56, v56, v104, s[82:83]\n\t"
        "v_cndmask_b32_e64 v64, v64, v106, s[82:83]\n\t"
        "v_cndmask_b32_e64 v57, v57, v108, s[84:85]\n\t"
        "v_cndmask_b32_e64 v65, v65, v110, s[84:85]\n\t"
        "v_cndmask_b32_e64 v58, v58, v112, s[86:87]\n\t"
        "v_cndmask_b32_e64 v66, v66, v114, s[86:87]\n\t"
        "v_cndmask_b32_e64 v59, v59, v116, s[88:89]\n\t"
        "v_cndmask_b32_e64 v67, v67, v118, s[88:89]\n\t"
        "v_cndmask_b32_e64 v76, v76, v79, s[90:91]\n\t"
        "v_cndmask_b32_e64 v77, v77, v80, s[90:91]\n\t"
        "s_cmp_eq_u64 vcc, exec\n\t"
        "s_cbranch_scc1 4f\n\t"
        "7:\n\t"
        "buffer_load_dwordx4 v[88:91], %[o0], %[d], 0 offen sc1\n\t"
        "buffer_load_dwordx4 v[92:95], %[o1], %[d], 0 offen sc1\n\t"
        "buffer_load_dwordx4 v[96:99], %[o2], %[d], 0 offen sc1\n\t"
        "buffer_load_dwordx4 v[100:103], %[o3], %[d], 0 offen sc1\n\t"
        "buffer_load_dwordx4 v[104:107], %[o4], %[d], 0 offen sc1\n\t"
        "buffer_load_dwordx4 v[108:111], %[o5], %[d], 0 offen sc1\n\t"
        "buffer_load_dwordx4 v[112:115], %[o6], %[d], 0 offen sc1\n\t"
        "buffer_load_dwordx4 v[116:119], %[o7], %[d], 0 offen sc1\n\t"
        "buffer_load_dwordx4 v[120:123], %[o8], %[d], 0 offen sc1\n\t"
        "buffer_load_dwordx4 v[124:127], %[o8], %[d], s73 offen sc1\n\t"
        "s_waitcnt vmcnt(10)\n\t"
        "v_bitop3_b32 v78, v129, v131, %[t] bitop3:0x7e\n\t"
        "v_bitop3_b32 v79, v133, v135, %[t] bitop3:0x7e\n\t"
        "v_bitop3_b32 v80, v137, v139, %[t] bitop3:0x7e\n\t"
        "v_bitop3_b32 v81, v141, v143, %[t] bitop3:0x7e\n\t"
        "v_bitop3_b32 v82, v145, v147, %[t] bitop3:0x7e\n\t"
        "v_bitop3_b32 v83, v149, v151, %[t] bitop3:0x7e\n\t"
        "v_bitop3_b32 v84, v153, v155, %[t] bitop3:0x7e\n\t"
        "v_bitop3_b32 v85, v157, v159, %[t] bitop3:0x7e\n\t"
        "v_bitop3_b32 v86, v161, v163, %[t] bitop3:0x7e\n\t"
        "v_bitop3_b32 v87, v165, v167, %[t] bitop3:0x7e\n\t"
        "v_or_b32 v86, v86, v87\n\t"
        "v_cmp_eq_u32 s[74:75], 0, v78\n\t"
        "v_cmp_eq_u32 s[76:77], 0, v79\n\t"
        "v_cmp_eq_u32 s[78:79], 0, v80\n\t"
        "v_cmp_eq_u32 s[80:81], 0, v81\n\t"
        "v_cmp_eq_u32 s[82:83], 0, v82\n\t"
        "v_cmp_eq_u32 s[84:85], 0, v83\n\t"
        "v_cmp_eq_u32 s[86:87], 0, v84\n\t"
        "v_cmp_eq_u32 s[88:89], 0, v85\n\t"
        "v_cmp_eq_u32 s[90:91], 0, v86\n\t"
        "v_cndmask_b32_e64 %[o0], %[o0], 2.0, s[74:75]\n\t"
        "v_cndmask_b32_e64 %[o1], %[o1], 2.0, s[76:77]\n\t"
        "v_cndmask_b32_e64 %[o2], %[o2], 2.0, s[78:79]\n\t"
        "v_cndmask_b32_e64 %[o3], %[o3], 2.0, s[80:81]\n\t"
        "v_cndmask_b32_e64 %[o4], %[o4], 2.0, s[82:83]\n\t"
        "v_cndmask_b32_e64 %[o5], %[o5], 2.0, s[84:85]\n\t"
        "v_cndmask_b32_e64 %[o6], %[o6], 2.0, s[86:87]\n\t"
        "v_cndmask_b32_e64 %[o7], %[o7], 2.0, s[88:89]\n\t"
        "v_cndmask_b32_e64 %[o8], %[o8], 2.0, s[90:91]\n\t"
        "v_min3_u32 v78, %[o0], %[o1], %[o2]\n\t"
        "v_min3_u32 v79, %[o3], %[o4], %[o5]\n\t"
        "v_min3_u32 v80, %[o6], %[o7], %[o8]\n\t"
        "v_min3_u32 v78, v78, v79, v80\n\t"
        "v_cmp_eq_u32 vcc, 2.0, v78\n\t"
        "v_add_f32 v79, v160, v164\n\t"
        "v_add_f32 v80, v162, v166\n\t"
        "v_cndmask_b32_e64 v52, v52, v128, s[74:75]\n\t"
        "v_cndmask_b32_e64 v60, v60, v130, s[74:75]\n\t"
        "v_cndmask_b32_e64 v53, v53, v132, s[76:77]\n\t"
        "v_cndmask_b32_e64 v61, v61, v134, s[76:77]\n\t"
        "v_cndmask_b32_e64 v54, v54, v136, s[78:79]\n\t"
        "v_cndmask_b32_e64 v62, v62, v138, s[78:79]\n\t"
        "v_cndmask_b32_e64 v55, v55, v140, s[80:81]\n\t"
        "v_cndmask_b32_e64 v63, v63, v142, s[80:81]\n\t"
        "v_cndmask_b32_e64 v56, v56, v144, s[82:83]\n\t"
        "v_cndmask_b32_e64 v64, v64, v146, s[82:83]\n\t"
        "v_cndmask_b32_e64 v57, v57, v148, s[84:85]\n\t"
        "v_cndmask_b32_e64 v65, v65, v150, s[84:85]\n\t"
        "v_cndmask_b32_e64 v58, v58, v152, s[86:87]\n\t"
        "v_cndmask_b32_e64 v66, v66, v154, s[86:87]\n\t"
        "v_cndmask_b32_e64 v59, v59, v156, s[88:89]\n\t"
        "v_cndmask_b32_e64 v67, v67, v158, s[88:89]\n\t"
        "v_cndmask_b32_e64 v76, v76, v79, s[90:91]\n\t"
        "v_cndmask_b32_e64 v77, v77, v80, s[90:91]\n\t"
        "s_cmp_eq_u64 vcc, exec\n\t"
        "s_cbranch_scc1 4f\n\t"
        "s_sub_u32 s72, s72, 1\n\t"
        "s_cbranch_scc0 3b\n\t"
        "4:\n\t"
        "v_add_f32 v68, v52, v53\n\t"
        "v_add_f32 v69, v54, v55\n\t"
        "v_add_f32 v70, v56, v57\n\t"
        "v_add_f32 v71, v58, v59\n\t"
        "v_add_f32 v72, v60, v61\n\t"
        "v_add_f32 v73, v62, v63\n\t"
        "v_add_f32 v74, v64, v65\n\t"
        "v_add_f32 v75, v66, v67\n\t"
        "s_branch 1b\n\t"
        "5:\n\t"
        "s_waitcnt vmcnt(0)"
        : [o0] "+v"(o[0]), [o1] "+v"(o[1]), [o2] "+v"(o[2]), [o3] "+v"(o[3]), [o4] "+v"(o[4]), [o5] "+v"(o[5]), [o6] "+v"(o[6]), [o7] "+v"(o[7]), [o8] "+v"(o[8])
        : [d] "s"(dws), [t] "v"(tag), [x] "v"(xres), [oo] "v"(oo), [oe] "v"(oe), [ro] "s"(dout)
        : "memory", "vcc", "scc", "s72", "s73", "s74", "s75", "s76", "s77", "s78", "s79", "s80", "s81", "s82", "s83", "s84", "s85", "s86", "s87", "s88", "s89", "s90", "s91", "v52", "v53", "v54", "v55", "v56", "v57", "v58", "v59", "v60", "v61", "v62", "v63", "v64", "v65", "v66", "v67", "v68", "v69", "v70", "v71", "v72", "v73", "v74", "v75", "v76", "v77", "v78", "v79", "v80", "v81", "v82", "v83", "v84", "v85", "v86", "v87", "v88", "v89", "v90", "v91", "v92", "v93", "v94", "v95", "v96", "v97", "v98", "v99", "v100", "v101", "v102", "v103", "v104", "v105", "v106", "v107", "v108", "v109", "v110", "v111", "v112", "v113", "v114", "v115", "v116", "v117", "v118", "v119", "v120", "v121", "v122", "v123", "v124", "v125", "v126", "v127", "v128", "v129", "v130", "v131", "v132", "v133", "v134", "v135", "v136", "v137", "v138", "v139", "v140", "v141", "v142", "v143", "v144", "v145", "v146", "v147", "v148", "v149", "v150", "v151", "v152", "v153", "v154", "v155", "v156", "v157", "v158", "v159", "v160", "v161", "v162", "v163", "v164", "v165", "v166", "v167");
    EXIT_WAVE
  }

  LANE_IDS
  const int w = wave;
  __builtin_amdgcn_s_setprio(2);
  const int rowl = 32 * (n >> 3) + 8 * g, chk = (n & 7) * 16;
  const int afo = ((n >> 2) & 1) * 64 + g * 16;
  f4 acc[8];
#pragma unroll
  for (int j = 0; j < 8; ++j) acc[j] = f4{0.f, 0.f, 0.f, 0.f};
  if (__builtin_expect(isS, 1)) {
    const u16* base = c < 64 ? wq + c * 64 : c < 136 ? wg + (c - 64) * 64 : wu + (c - 136) * 64;
    const int stride = c < 64 ? NQ * 2 : FF * 2;
    const __amdgpu_buffer_rsrc_t rw = mkrsrc(base, (DM - 1) * stride + 128);
    const int voff = rowl * stride + chk;
    const unsigned tbase = tstart + (unsigned)(((2 * w - AHEAD) * TAU) >> 8);
    v4u R[24];
#pragma unroll
    for (int i = 0; i < 24; ++i) {
      if ((i & 1) == 0 && 3 * i > AHEAD) PACE(3 * i);
      R[i] = ld_nt(rw, voff, (192 * w + 64 * (i >> 3) + (i & 7)) * stride);
    }
    __builtin_amdgcn_sched_barrier(0);
    lds_wait<1>(FL + F_XN, 1);
#pragma unroll
    for (int gi = 0; gi < 3; ++gi)
      gemv_group(R + 8 * gi, *(const LAS v4u*)(L + L_XN + (n & 3) * XN_STR + (3 * w + gi) * 128 + afo), acc);
    *(LAS f4*)(L + L_S1R + w * 1024 + lane * 16) = gemv_finish<false>(acc, lane);
    const unsigned tag = FL[F_TAG];
    if (lds_arrive(F_S1) == 2) {
      const f4 o = (*(const LAS f4*)(L + L_S1R + lane * 16) + *(const LAS f4*)(L + L_S1R + 1024 + lane * 16)) + *(const LAS f4*)(L + L_S1R + 2048 + lane * 16);
      st_sc1(v4u{cvtpk(o[0], o[1]), tag, cvtpk(o[2], o[3]), tag}, rws,
             WS_S1 + (2 * (n >> 3) + (g & 1)) * S1_ROW + (64 * c + 8 * (n & 7) + 4 * (g >> 1)) * 4);
    }
    if (c >= N_WO) EXIT_WAVE
    lds_wait<1>(FL + F_D, WOCNT);
    const __amdgpu_buffer_rsrc_t ro = mkrsrc(wo + (size_t)ksl * 64 * DM + (3 * cs + w) * 64, 63 * DM * 2 + 128);
    v4u W[8], T[8];
#pragma unroll
    for (int i = 0; i < 8; ++i) W[i] = ld_nt(ro, rowl * (DM * 2) + chk + i * (DM * 2));
#pragma unroll
    for (int q = 0; q < 4; ++q)
#pragma unroll
      for (int p = 0; p < 4; ++p) {
        T[2 * q][p] = __builtin_amdgcn_perm(W[2 * p + 1][q], W[2 * p][q], 0x05040100u);
        T[2 * q + 1][p] = __builtin_amdgcn_perm(W[2 * p + 1][q], W[2 * p][q], 0x07060302u);
      }
    int rec = WS_H2 + (cs * 64 + ksl) * 6144 + w * 2048 + n * 64 + g * 16;
    asm volatile("" : "+v"(T[0]), "+v"(T[1]), "+v"(T[2]), "+v"(T[3]), "+v"(T[4]), "+v"(T[5]), "+v"(T[6]), "+v"(T[7]), "+v"(rec));
    v4u alo, ahi;
    {
      const int af = L_AF + lane * 16;
      unsigned f_, s_;
      asm volatile("s_mov_b32 s72, %7\n1:\n\tds_read_b32 %2, %4\n\tds_read_b128 %0, %5\n\tds_read_b128 %1, %6\n\ts_waitcnt lgkmcnt(2)\n\t"
                   "v_readfirstlane_b32 %3, %2\n\ts_cmp_lg_u32 %3, 0\n\ts_cbranch_scc1 2f\n\ts_sub_u32 s72, s72, 1\n\ts_cbranch_scc0 1b\n2:\n\ts_waitcnt lgkmcnt(0)"
                   : "=&v"(alo), "=&v"(ahi), "=&v"(f_), "=&s"(s_)
                   : "v"(L_FLAG + 4 * F_A), "v"(n < 8 ? af : L_ZERO), "v"(n < 8 ? L_ZERO : af), "i"(LDS_SPIN) : "memory", "scc", "s72");
    }
#pragma unroll
    for (int j = 0; j < 4; ++j) acc[j] = mfma(alo, T[j], f4{0.f, 0.f, 0.f, 0.f});
#pragma unroll
    for (int j = 0; j < 4; ++j) acc[j] = mfma(ahi, T[j + 4], acc[j]);
    const f4 G = gemv_fold2(acc, lane);
    st_sc1(v4u{__float_as_uint(G[0]), tag, __float_as_uint(G[1]), tag}, rws, rec);
    st_sc1(v4u{__float_as_uint(G[2]), tag, __float_as_uint(G[3]), tag}, rws, rec + 1024);
    lds_post(F_H2);
  } else {
    const int ds = (c - N_S1) % 3, rs = (c - N_S1) / 3;
    const __amdgpu_buffer_rsrc_t rd = mkrsrc(wd + (size_t)rs * 288 * DM + (3 * ds + w) * 64, 287 * DM * 2 + 128);
    const int voff = rowl * (DM * 2) + chk, voff4 = n >= 8 ? OOB : voff;
    v4u R[24];
#pragma unroll
    for (int i = 0; i < 8; ++i) R[i] = ld_nt(rd, voff, i * (DM * 2));
    __builtin_amdgcn_sched_barrier(0);
    lds_wait<1>(FL + F_ISS, 8);
    const unsigned tb2 = (unsigned)__builtin_amdgcn_s_memrealtime() + (unsigned)((2 * w * TAUD) >> 8);
#define PACE_D(i) if (((i) & 1) == 0 && 3 * (i) > AHD) wait_until((int)(((3 * (i) - AHD) * TAUD) >> 8), tb2);
#pragma unroll
    for (int i = 8; i < 24; ++i) { PACE_D(i - 8) R[i] = ld_nt(rd, voff, (64 * (i >> 3) + (i & 7)) * (DM * 2)); }
    __builtin_amdgcn_sched_barrier(0);
    lds_wait<1>(FL + F_H, 1);
    const unsigned tag = FL[F_TAG];
    const int ao = L_H + (n & 3) * H_STR + afo;
    gemv_group(R, *(const LAS v4u*)(L + ao), acc);
    __builtin_amdgcn_sched_barrier(0);
#pragma unroll
    for (int i = 0; i < 8; ++i) { PACE_D(16 + i) R[i] = ld_nt(rd, voff, (192 + i) * (DM * 2)); }
    __builtin_amdgcn_sched_barrier(0);
    gemv_group(R + 8, *(const LAS v4u*)(L + ao + 128), acc);
    __builtin_amdgcn_sched_barrier(0);
#pragma unroll
    for (int i = 0; i < 8; ++i) R[8 + i] = ld_nt(rd, voff4, (256 + i) * (DM * 2));
    __builtin_amdgcn_sched_barrier(0);
    gemv_group(R + 16, *(const LAS v4u*)(L + ao + 256), acc);
    gemv_group(R, *(const LAS v4u*)(L + ao + 384), acc);
    gemv_group(R + 8, *(const LAS v4u*)(L + ao + 512), acc);
    const f4 G = gemv_finish<true>(acc, lane);
    const int rec = WS_F2 + (ds * 16 + rs) * 6144 + w * 2048 + n * 64 + g * 16;
    st_sc1(v4u{__float_as_uint(G[0]), tag, __float_as_uint(G[1]), tag}, rws, rec);
    st_sc1(v4u{__float_as_uint(G[2]), tag, __float_as_uint(G[3]), tag}, rws, rec + 1024);
  }
}

extern "C" void kernel_launch(void* const* d_in, const int* in_sizes, int n_in, void* d_out, int out_size, void* d_ws,
                              size_t ws_size, hipStream_t stream) {
  fused_decode<<<NCU, NTHR, 0, stream>>>((const u16*)d_in[0], (const u16*)d_in[1], (const u16*)d_in[2], (const u16*)d_in[3],
                                         (const u16*)d_in[4], (const u16*)d_in[6], (const u16*)d_in[7], (const u16*)d_in[8],
                                         (u16*)d_out, (unsigned char*)d_ws);
}
```
